```python
import torch

def _softmax(x, axis=-1):
    return torch.softmax(x, dim=axis)
def _gelu(x):
    return torch.nn.functional.gelu(x, approximate="tanh")
def _silu(x):
    return torch.nn.functional.silu(x)
def _sigmoid(x):
    return torch.sigmoid(x)
def _layernorm(x, axis=-1, eps=1e-5):
    m = x.mean(dim=axis, keepdim=True)
    v = x.var(dim=axis, keepdim=True, unbiased=False)
    return (x - m) / torch.sqrt(v + eps)
def _scatter_add(tgt, idx, src, axis=0):
    return tgt.clone().index_add_(axis, idx, src)
def _segment_sum(src, seg, n):
    out = torch.zeros((n,) + src.shape[1:], dtype=src.dtype)
    return out.index_add_(0, seg, src)
def _pad(x, pw):
    flat = tuple(v for pair in reversed(pw) for v in pair)
    return torch.nn.functional.pad(x, flat)

kernel_name = 'standalone__kv_cache_quant_int4_int2_kv_with_per_channel_or_per_token_scales__prompt'

def setup_inputs():

    _w = (lambda a: (lambda t: t.to(torch.bfloat16) if t.is_floating_point() else t)(torch.as_tensor(a)))
    B = 1
    Sq = 2048
    Skv = 2048
    Hq = 32
    Hkv = 8
    Dh = 128
    bits = 8
    G = 64
    if G <= 0 or G >= Skv:
        G = max(1, Skv // 2)
    Skv = (Skv // G) * G
    ngk = Skv // G
    qmax = (1 << bits) - 1
    return {
        "q": _w((lambda *s: torch.randn(*s, dtype=torch.bfloat16) * 0.2)(B, Hq, Sq, Dh)),
        "qk": _w(torch.randint(0, qmax + 1, (B, Hkv, Skv, Dh)).to(torch.int32)),
        "qv": _w(torch.randint(0, qmax + 1, (B, Hkv, Skv, Dh)).to(torch.int32)),
        "k_scales": _w(torch.abs((lambda *s: torch.randn(*s, dtype=torch.bfloat16) * 0.2)(B, Hkv, ngk, Dh)) * 0.05 + 0.02),
        "k_zeros": _w((lambda *s: torch.randn(*s, dtype=torch.bfloat16) * 0.2)(B, Hkv, ngk, Dh) + float(qmax) / 2.0),
        "v_scales": _w(torch.abs((lambda *s: torch.randn(*s, dtype=torch.bfloat16) * 0.05)(B, Hkv, Skv, 1)) * 0.05 + 0.02),
        "v_zeros": _w((lambda *s: torch.randn(*s, dtype=torch.bfloat16) * 0.05)(B, Hkv, Skv, 1) + float(qmax) / 2.0),
    }

def reference(q, qk, qv, k_scales, k_zeros, v_scales, v_zeros):
    B, Hq, Sq, Dh = q.shape
    Hkv, Skv = qk.shape[1], qk.shape[2]
    ngk = k_scales.shape[2]
    G = Skv // ngk
    ks = (lambda x, n, axis: torch.repeat_interleave(x, n, dim=axis))(k_scales, G, axis=2)
    kz = (lambda x, n, axis: torch.repeat_interleave(x, n, dim=axis))(k_zeros, G, axis=2)
    k = ((lambda x, dt: x.to(dt))(qk, q.dtype) - kz) * ks
    v = ((lambda x, dt: x.to(dt))(qv, q.dtype) - v_zeros) * v_scales
    rep = Hq // Hkv
    if rep > 1:
        k = (lambda x, n, axis: torch.repeat_interleave(x, n, dim=axis))(k, rep, axis=1)
        v = (lambda x, n, axis: torch.repeat_interleave(x, n, dim=axis))(v, rep, axis=1)
    scale = float(Dh) ** -0.5
    logits = torch.einsum("bhqd,bhkd->bhqk", q, k) * scale
    p = _softmax(logits, axis=-1)
    o = torch.einsum("bhqk,bhkd->bhqd", p, v)
    return o
```

```cpp
#include <hip/hip_runtime.h>
#include <hip/hip_bf16.h>
using bf16 = __hip_bfloat16;
constexpr int   D = 128, NW = 8, QBLK = 32, KVBLK = 64;
constexpr float SCALE = 0.088388347648318440f;
constexpr float THR = 8.f;
constexpr int SDEPTH = 2;
constexpr int B = 1, HQ = 32, HKV = 8, S = 2048, SKV = 2048;
constexpr int LDQ = D, LDK = D, LDO = D;
constexpr size_t SHM_V = KVBLK * D * 2, SHM_K = KVBLK * D * 2, SHM_ATTN = 2 * SHM_V + 2 * SHM_K + NW * 64 * 4;
using bf16x8 = __attribute__((ext_vector_type(8))) short;
using s16x4  = __attribute__((ext_vector_type(4))) short;
using f32x16 = __attribute__((ext_vector_type(16))) float;
using u32x4  = __attribute__((ext_vector_type(4))) unsigned;
using i32x4  = __attribute__((ext_vector_type(4))) int;
#define KSWZ(row, colB) ((row) * 256 + ((colB) ^ (((row) & 7) << 4)))
#define SBAR() __builtin_amdgcn_sched_barrier(0)
__device__ __forceinline__ int crow(int r, int hi) { return (r & 3) + 8 * (r >> 2) + 4 * hi; }
__device__ __forceinline__ unsigned cvtpk(float lo, float hi) {
  unsigned r; asm volatile("v_cvt_pk_bf16_f32 %0, %1, %2" : "=v"(r) : "v"(lo), "v"(hi)); return r;
}
__device__ __forceinline__ bf16x8 ld8(const bf16* p) { return *reinterpret_cast<const bf16x8*>(p); }
__device__ __forceinline__ void partialSM(f32x16& p0, f32x16& p1, float& m_reg, float& mn, float& alpha) {
  constexpr float C = SCALE * 1.4426950408889634f;
  float pmax = p0[0]; for (int r = 1; r < 16; ++r) pmax = fmaxf(pmax, p0[r]); for (int r = 0; r < 16; ++r) pmax = fmaxf(pmax, p1[r]);
  { auto rr = __builtin_amdgcn_permlane32_swap(__float_as_uint(pmax), __float_as_uint(pmax), false, false);
    pmax = fmaxf(__uint_as_float(rr[0]), __uint_as_float(rr[1])); }
  if (__builtin_expect(__all(pmax - m_reg <= THR / SCALE), 1)) { mn = m_reg; alpha = 1.f; }
  else { mn = fmaxf(m_reg, pmax); alpha = __builtin_amdgcn_exp2f((m_reg - mn) * C); m_reg = mn; }
  float mnC = -mn * C;
  for (int r = 0; r < 16; ++r) p0[r] = fmaf(p0[r], C, mnC); for (int r = 0; r < 16; ++r) p1[r] = fmaf(p1[r], C, mnC);
  for (int r = 0; r < 16; ++r) p0[r] = __builtin_amdgcn_exp2f(p0[r]);
}
__device__ __forceinline__ void finishSM(f32x16& p0, f32x16& p1, float alpha, float& l_reg, bf16x8& pa0, bf16x8& pa1, bf16x8& pa2, bf16x8& pa3) {
  for (int r = 0; r < 16; ++r) p1[r] = __builtin_amdgcn_exp2f(p1[r]);
  float ps = 0; for (int r = 0; r < 16; ++r) ps += p0[r]; for (int r = 0; r < 16; ++r) ps += p1[r];
  { auto rr = __builtin_amdgcn_permlane32_swap(__float_as_uint(ps), __float_as_uint(ps), false, false);
    ps = __uint_as_float(rr[0]) + __uint_as_float(rr[1]); }
  l_reg = l_reg * alpha + ps;
#define PK4(P, BASE, OUT) do { unsigned a0 = cvtpk(P[BASE + 0], P[BASE + 1]), a1 = cvtpk(P[BASE + 2], P[BASE + 3]);   \
    unsigned b0 = cvtpk(P[BASE + 4], P[BASE + 5]), b1 = cvtpk(P[BASE + 6], P[BASE + 7]);                              \
    auto r0 = __builtin_amdgcn_permlane32_swap(a0, b0, false, false); auto r1 = __builtin_amdgcn_permlane32_swap(a1, b1, false, false); \
    u32x4 w = {r0[0], r1[0], r0[1], r1[1]}; OUT = *reinterpret_cast<bf16x8*>(&w); } while (0)
  PK4(p0, 0, pa0); PK4(p0, 8, pa1); PK4(p1, 0, pa2); PK4(p1, 8, pa3);
#undef PK4
}
__device__ __forceinline__ void qkt(f32x16& p0, f32x16& p1, const bf16* Ks, const bf16x8* qr, int r32, int hi) {
  p0 = f32x16{}; p1 = f32x16{};
  for (int d0 = 0; d0 < 8; ++d0) { int cb = (d0 * 16 + hi * 8) * 2;
    bf16x8 b0 = *reinterpret_cast<const bf16x8*>((const char*)Ks + KSWZ(r32, cb));
    bf16x8 b1 = *reinterpret_cast<const bf16x8*>((const char*)Ks + KSWZ(32 + r32, cb));
    p0 = __builtin_amdgcn_mfma_f32_32x32x16_bf16(b0, qr[d0], p0, 0, 0, 0);
    p1 = __builtin_amdgcn_mfma_f32_32x32x16_bf16(b1, qr[d0], p1, 0, 0, 0); }
}
__device__ __forceinline__ int v_st(int k, int c) { const int kk = (k & ~0xC) | ((k & 4) << 1) | ((k & 8) >> 1); return ((kk >> 3) * 4 + (c >> 5)) * 512 + ((kk & 7) * 32 + (c & 31)) * 2; }
__device__ __forceinline__ int v_rd_base(int lane) { return ((lane & 3) << 3) | (((lane >> 2) & 3) << 6) | (((lane >> 4) & 1) << 5) | (((lane >> 5) & 1) << 8); }
constexpr int v_rd_off(int d0, int ks, int half) { return d0 * 512 + ks * 4096 + half * 2048; }
template <int OFF> __device__ __forceinline__ s16x4 tr_read(int vb) {
  s16x4 r; asm volatile("ds_read_b64_tr_b16 %0, %1 offset:%2" : "=&v"(r) : "v"(vb), "i"(OFF) : "memory"); return r;
}
template <int D0> __device__ __forceinline__ void pv_one(f32x16& od, int vb, bf16x8 pa0, bf16x8 pa1, bf16x8 pa2, bf16x8 pa3) {
  const s16x4 l0 = tr_read<v_rd_off(D0, 0, 0)>(vb), h0 = tr_read<v_rd_off(D0, 0, 1)>(vb), l1 = tr_read<v_rd_off(D0, 1, 0)>(vb), h1 = tr_read<v_rd_off(D0, 1, 1)>(vb);
  const s16x4 l2 = tr_read<v_rd_off(D0, 2, 0)>(vb), h2 = tr_read<v_rd_off(D0, 2, 1)>(vb), l3 = tr_read<v_rd_off(D0, 3, 0)>(vb), h3 = tr_read<v_rd_off(D0, 3, 1)>(vb);
  asm volatile("s_waitcnt lgkmcnt(0)" ::: "memory"); SBAR();
#define PK(L, H) (bf16x8){L[0], L[1], L[2], L[3], H[0], H[1], H[2], H[3]}
  od = __builtin_amdgcn_mfma_f32_32x32x16_bf16(pa0, PK(l0, h0), od, 0, 0, 0);
  od = __builtin_amdgcn_mfma_f32_32x32x16_bf16(pa1, PK(l1, h1), od, 0, 0, 0);
  od = __builtin_amdgcn_mfma_f32_32x32x16_bf16(pa2, PK(l2, h2), od, 0, 0, 0);
  od = __builtin_amdgcn_mfma_f32_32x32x16_bf16(pa3, PK(l3, h3), od, 0, 0, 0);
#undef PK
}
__device__ __forceinline__ void pv_d0(f32x16* o, int vb, bf16x8 pa0, bf16x8 pa1, bf16x8 pa2, bf16x8 pa3) {
  pv_one<0>(o[0], vb, pa0, pa1, pa2, pa3); pv_one<1>(o[1], vb, pa0, pa1, pa2, pa3); pv_one<2>(o[2], vb, pa0, pa1, pa2, pa3); pv_one<3>(o[3], vb, pa0, pa1, pa2, pa3);
}
__device__ __forceinline__ void attn_dense_body(const bf16* __restrict__ Qb, const bf16* __restrict__ Kh, const bf16* __restrict__ Vh,
                                                bf16* __restrict__ Ob, int seq, char* lds) {
  const int tid = threadIdx.x, wid = tid >> 6, lane = tid & 63, r32 = lane & 31, hi = lane >> 5;
  bf16* V_lds = (bf16*)lds; bf16* K_lds = (bf16*)(lds + 2 * SHM_V);
  float* ws = (float*)(lds + 2 * SHM_V + 2 * SHM_K) + wid * 64; float* li_l = ws; float* al_l = ws + 32;
  float m_reg = -1e30f, l_reg = 0; f32x16 o[4] = {}; bf16x8 qr[8];
  const bf16* Qw = Qb + (long)(wid * QBLK + r32) * LDQ + hi * 8;
#pragma unroll
  for (int d0 = 0; d0 < 8; ++d0) qr[d0] = ld8(Qw + d0 * 16);
  const int sr = tid >> 4, sc = (tid & 15) * 8, vst0 = v_st(sr, sc), vst1 = v_st(32 + sr, sc);
  const int vb0 = (int)(uintptr_t)V_lds + v_rd_base(lane);
  struct { bf16x8 vs0, vs1, ks0, ks1; } sr_[SDEPTH];
#define SLOAD(i, k0) do { sr_[i].vs0 = ld8(&Vh[(long)((k0) + sr) * LDK + sc]); sr_[i].vs1 = ld8(&Vh[(long)((k0) + 32 + sr) * LDK + sc]); \
    sr_[i].ks0 = ld8(&Kh[(long)((k0) + sr) * LDK + sc]); sr_[i].ks1 = ld8(&Kh[(long)((k0) + 32 + sr) * LDK + sc]); } while (0)
#define SWRITE(b, i) do { *(bf16x8*)((char*)V_lds + (b) * SHM_V + vst0) = (sr_[i].vs0);          \
    *(bf16x8*)((char*)V_lds + (b) * SHM_V + vst1) = (sr_[i].vs1); int kc = sc * 2;               \
    *(bf16x8*)((char*)K_lds + (b) * SHM_K + KSWZ(sr, kc)) = (sr_[i].ks0);                       \
    *(bf16x8*)((char*)K_lds + (b) * SHM_K + KSWZ(32 + sr, kc)) = (sr_[i].ks1); } while (0)
#define SWAIT() do { asm volatile("s_waitcnt vmcnt(4)" ::: "memory"); } while (0)
#define RESC(a) do { if (__any((a) < 1.f)) { if (hi == 0) al_l[r32] = (a); asm volatile("s_waitcnt lgkmcnt(0)" ::: "memory"); \
    for (int d = 0; d < 4; ++d) for (int r = 0; r < 16; ++r) o[d][r] *= al_l[crow(r, hi)]; } } while (0)
  f32x16 pA0, pA1, pB0, pB1; float mnA, mnB, alA, alB; bf16x8 pa0, pa1, pa2, pa3; const int NT = seq / KVBLK;
  constexpr int SE = 0, SO = SDEPTH - 1;
  SLOAD(SE, 0); asm volatile("s_waitcnt vmcnt(0)" ::: "memory"); SWRITE(0, SE); __syncthreads();
  qkt(pA0, pA1, K_lds, qr, r32, hi); partialSM(pA0, pA1, m_reg, mnA, alA);
  SLOAD(SO, KVBLK); if (2 < NT) SLOAD(SE, 2 * KVBLK);
  SWAIT(); SWRITE(1, SO); __syncthreads();
  for (int j = 1; j + 1 < NT; j += 2) {
    SBAR(); qkt(pB0, pB1, (bf16*)((char*)K_lds + SHM_K), qr, r32, hi);
    finishSM(pA0, pA1, alA, l_reg, pa0, pa1, pa2, pa3); SBAR();
    SLOAD(SO, (j + SDEPTH) * KVBLK); SBAR();
    pv_d0(o, vb0, pa0, pa1, pa2, pa3); partialSM(pB0, pB1, m_reg, mnB, alB);
    __syncthreads(); SWAIT(); SWRITE(0, SE);
    RESC(alB); __syncthreads();
    SBAR(); qkt(pA0, pA1, K_lds, qr, r32, hi);
    finishSM(pB0, pB1, alB, l_reg, pa0, pa1, pa2, pa3); SBAR();
    if (j + 3 < NT) SLOAD(SE, (j + 1 + SDEPTH) * KVBLK); SBAR();
    pv_d0(o, vb0 + (int)SHM_V, pa0, pa1, pa2, pa3); partialSM(pA0, pA1, m_reg, mnA, alA);
    __syncthreads(); SWAIT(); SWRITE(1, SO);
    RESC(alA); __syncthreads();
  }
  SBAR(); qkt(pB0, pB1, (bf16*)((char*)K_lds + SHM_K), qr, r32, hi);
  finishSM(pA0, pA1, alA, l_reg, pa0, pa1, pa2, pa3); SBAR();
  pv_d0(o, vb0, pa0, pa1, pa2, pa3); partialSM(pB0, pB1, m_reg, mnB, alB);
  __syncthreads(); RESC(alB);
  finishSM(pB0, pB1, alB, l_reg, pa0, pa1, pa2, pa3); SBAR();
  pv_d0(o, vb0 + (int)SHM_V, pa0, pa1, pa2, pa3);
  if (hi == 0) li_l[r32] = l_reg; asm volatile("s_waitcnt lgkmcnt(0)" ::: "memory");
  float rli[16];
#pragma unroll
  for (int r = 0; r < 16; ++r) rli[r] = __builtin_amdgcn_rcpf(li_l[crow(r, hi)]);
  bf16* Ow = Ob + (long)(wid * QBLK) * LDO;
#pragma unroll
  for (int r = 0; r < 16; ++r) { int orow = crow(r, hi);
    for (int d0 = 0; d0 < 4; ++d0) Ow[(long)orow * LDO + d0 * 32 + r32] = __float2bfloat16(o[d0][r] * rli[r]); }
}
__global__ __launch_bounds__(64 * NW, 1)
void attn_fwd_dense(const bf16* __restrict__ Q, const bf16* __restrict__ K, const bf16* __restrict__ V, bf16* __restrict__ O) {
  extern __shared__ __attribute__((aligned(16))) char shm[];
  const int bid = blockIdx.x; const int kvh = bid & 7; const int rest = bid >> 3;
  const int h = kvh * 4 + (rest & 3); const int qb = rest >> 2;
  const long q0 = ((long)h * S + (long)qb * 256) * D, k0 = (long)kvh * SKV * D;
  attn_dense_body(Q + q0, K + k0, V + k0, O + q0, SKV, shm);
}
__device__ __forceinline__ float bf2f(unsigned short u) { return __uint_as_float((unsigned)u << 16); }
__device__ __forceinline__ float rbf(float x) { return __bfloat162float(__float2bfloat16(x)); }
__global__ __launch_bounds__(256) void dequant(const int* __restrict__ qk, const int* __restrict__ qv,
    const unsigned short* __restrict__ ks, const unsigned short* __restrict__ kz,
    const unsigned short* __restrict__ vs, const unsigned short* __restrict__ vz,
    bf16* __restrict__ kb, bf16* __restrict__ vb) {
  const int n8 = HKV * SKV * D / 8;
  int i = blockIdx.x * 256 + threadIdx.x;
  const bool isK = i < n8; const int j = isK ? i : i - n8;
  const int row = j >> 4, c = (j & 15) * 8;
  const int* src = (isK ? qk : qv) + (long)j * 8;
  i32x4 a = *(const i32x4*)src, b = *(const i32x4*)(src + 4);
  float x[8] = {(float)a[0], (float)a[1], (float)a[2], (float)a[3], (float)b[0], (float)b[1], (float)b[2], (float)b[3]};
  float s[8], z[8];
  if (isK) {
    const int g = row >> 6;
    bf16x8 sv = *(const bf16x8*)(ks + (long)g * D + c), zv = *(const bf16x8*)(kz + (long)g * D + c);
    for (int e = 0; e < 8; ++e) { s[e] = bf2f((unsigned short)sv[e]); z[e] = bf2f((unsigned short)zv[e]); }
  } else {
    float s1 = bf2f(vs[row]), z1 = bf2f(vz[row]);
    for (int e = 0; e < 8; ++e) { s[e] = s1; z[e] = z1; }
  }
  float r[8];
  for (int e = 0; e < 8; ++e) r[e] = rbf(rbf(x[e]) - z[e]) * s[e];
  u32x4 w = {cvtpk(r[0], r[1]), cvtpk(r[2], r[3]), cvtpk(r[4], r[5]), cvtpk(r[6], r[7])};
  *(u32x4*)((isK ? kb : vb) + (long)j * 8) = w;
}
extern "C" void kernel_launch(void* const* d_in, const int* in_sizes, int n_in, void* d_out, int out_size, void* d_ws, size_t ws_size, hipStream_t stream) {
  constexpr long NK = (long)HKV * SKV * D;
  hipFuncSetAttribute((const void*)attn_fwd_dense, hipFuncAttributeMaxDynamicSharedMemorySize, (int)SHM_ATTN);
  bf16* kb = (bf16*)d_ws; bf16* vb = kb + NK;
  hipLaunchKernelGGL(dequant, dim3(2 * NK / 8 / 256), dim3(256), 0, stream, (const int*)d_in[1], (const int*)d_in[2],
     (const unsigned short*)d_in[3], (const unsigned short*)d_in[4], (const unsigned short*)d_in[5], (const unsigned short*)d_in[6], kb, vb);
  hipLaunchKernelGGL(attn_fwd_dense, dim3(256), dim3(64 * NW), SHM_ATTN, stream,
                     (const bf16*)d_in[0], (const bf16*)kb, (const bf16*)vb, (bf16*)d_out);
}
```

```cpp
#include <hip/hip_runtime.h>
#include <hip/hip_bf16.h>

constexpr int HQ = 32, HKV = 8, SQ = 2048, SKV = 2048, DH = 128;

__global__ __launch_bounds__(256, 1) void attn_fwd(const char* __restrict__ Qp, const char* __restrict__ qk, const char* __restrict__ qv,
    const char* __restrict__ ks, const char* __restrict__ kz, const char* __restrict__ vs, const char* __restrict__ vz,
    char* __restrict__ WS, char* __restrict__ Op) {
  __shared__ __attribute__((aligned(1024))) char lds[163840];
  const unsigned bid = blockIdx.x, kvh = bid & 7u, j = bid >> 3;
  const unsigned hq = kvh * 4u + (j & 3u), qblk = j >> 2;
  const unsigned wave = __builtin_amdgcn_readfirstlane(threadIdx.x >> 6);
  const size_t rowoff = ((size_t)hq * SQ + qblk * 256u + wave * 64u) * 256u;
  const char* q = Qp + rowoff;
  char* o = Op + rowoff;
  char* kv = WS + (size_t)kvh * (1 << 20);
  char* ctl = WS + (16u << 20) + kvh * 2048u;
  const unsigned key0 = kvh * SKV + (wave >> 1) * 32u;
  const size_t so = (size_t)key0 * 512u, ko = (size_t)(kvh * 32u) * 256u, vo = (size_t)key0 * 2u;
  const unsigned ldsb = (unsigned)(size_t)(__attribute__((address_space(3))) char*)lds;
  asm volatile(
    "s_mov_b32 s59, %[wave]\n"
    "s_mov_b32 s61, %[j]\n"
    "s_mov_b32 s60, %[lds]\n"
    "v_mbcnt_lo_u32_b32 v204, -1, 0\n"
    "v_mbcnt_hi_u32_b32 v204, -1, v204\n"
    "s_bitcmp1_b32 s59, 0\n"
    "s_cselect_b64 s[72:73], %[srcv], %[srck]\n"
    "s_cselect_b64 s[74:75], %[scv], %[sck]\n"
    "s_cselect_b64 s[76:77], %[zrv], %[zrk]\n"
    "s_cselect_b32 s78, 7, 8\n"
    "s_lshl_b32 s56, s61, s78\n"
    "s_add_u32 s74, s74, s56\n"
    "s_addc_u32 s75, s75, 0\n"
    "s_add_u32 s76, s76, s56\n"
    "s_addc_u32 s77, s77, 0\n"
    "s_mov_b32 s42, 1048576\n"
    "s_lshl_b32 s79, s59, 14\n"
    "s_add_u32 s79, s79, s60\n"
    "s_add_u32 s79, s79, 0x18000\n"
    "v_lshlrev_b32 v198, 4, v204\n"
    "v_lshrrev_b32 v207, 5, v204\n"
    "v_lshlrev_b32 v207, 6, v207\n"
    "v_xor_b32 v250, v198, v207\n"
    "v_and_b32 v205, 15, v204\n"
    "v_lshrrev_b32 v206, 4, v204\n"
    "s_sub_u32 s56, s78, 4\n"
    "v_lshlrev_b32 v251, s56, v206\n"
    "s_bitcmp1_b32 s59, 0\n"
    "s_cbranch_scc1 .Lrole_v\n"
    "global_load_dwordx4 v[64:67], v251, s[74:75] offset:0\n"
    "global_load_dwordx4 v[80:83], v251, s[76:77] offset:0\n"
    "global_load_dwordx4 v[68:71], v251, s[74:75] offset:64\n"
    "global_load_dwordx4 v[84:87], v251, s[76:77] offset:64\n"
    "global_load_dwordx4 v[72:75], v251, s[74:75] offset:128\n"
    "global_load_dwordx4 v[88:91], v251, s[76:77] offset:128\n"
    "global_load_dwordx4 v[76:79], v251, s[74:75] offset:192\n"
    "global_load_dwordx4 v[92:95], v251, s[76:77] offset:192\n"
    "s_lshl_b32 s56, s61, 15\n"
    "s_add_u32 s80, s72, s56\n"
    "s_addc_u32 s81, s73, 0\n"
    "s_mov_b32 m0, s79\n"
    "v_xor_b32 v248, 0x0, v250\n"
    "global_load_lds_dwordx4 v248, s[80:81] offset:0 nt\n"
    "v_xor_b32 v249, 0x20, v250\n"
    "global_load_lds_dwordx4 v249, s[80:81] offset:1024 nt\n"
    "v_xor_b32 v248, 0x10, v250\n"
    "global_load_lds_dwordx4 v248, s[80:81] offset:2048 nt\n"
    "v_xor_b32 v249, 0x30, v250\n"
    "global_load_lds_dwordx4 v249, s[80:81] offset:3072 nt\n"
    "s_add_u32 s80, s80, 0x1000\n"
    "s_addc_u32 s81, s81, 0\n"
    "s_add_u32 m0, m0, 0x1000\n"
    "v_xor_b32 v248, 0x80, v250\n"
    "global_load_lds_dwordx4 v248, s[80:81] offset:0 nt\n"
    "v_xor_b32 v249, 0xa0, v250\n"
    "global_load_lds_dwordx4 v249, s[80:81] offset:1024 nt\n"
    "v_xor_b32 v248, 0x90, v250\n"
    "global_load_lds_dwordx4 v248, s[80:81] offset:2048 nt\n"
    "v_xor_b32 v249, 0xb0, v250\n"
    "global_load_lds_dwordx4 v249, s[80:81] offset:3072 nt\n"
    "s_add_u32 s80, s80, 0x1000\n"
    "s_addc_u32 s81, s81, 0\n"
    "s_add_u32 m0, m0, 0x1000\n"
    "v_xor_b32 v248, 0x0, v250\n"
    "global_load_lds_dwordx4 v248, s[80:81] offset:0 nt\n"
    "v_xor_b32 v249, 0x20, v250\n"
    "global_load_lds_dwordx4 v249, s[80:81] offset:1024 nt\n"
    "v_xor_b32 v248, 0x10, v250\n"
    "global_load_lds_dwordx4 v248, s[80:81] offset:2048 nt\n"
    "v_xor_b32 v249, 0x30, v250\n"
    "global_load_lds_dwordx4 v249, s[80:81] offset:3072 nt\n"
    "s_add_u32 s80, s80, 0x1000\n"
    "s_addc_u32 s81, s81, 0\n"
    "s_add_u32 m0, m0, 0x1000\n"
    "v_xor_b32 v248, 0x80, v250\n"
    "global_load_lds_dwordx4 v248, s[80:81] offset:0 nt\n"
    "v_xor_b32 v249, 0xa0, v250\n"
    "global_load_lds_dwordx4 v249, s[80:81] offset:1024 nt\n"
    "v_xor_b32 v248, 0x90, v250\n"
    "global_load_lds_dwordx4 v248, s[80:81] offset:2048 nt\n"
    "v_xor_b32 v249, 0xb0, v250\n"
    "global_load_lds_dwordx4 v249, s[80:81] offset:3072 nt\n"
    "v_mov_b32 v254, 0\n"
    "v_mov_b32 v255, 0\n"
    "s_lshl_b32 s56, s59, 7\n"
    "v_mov_b32 v253, s56\n"
    "s_mov_b64 exec, 1\n"
    "global_atomic_add_x2 v[202:203], v253, v[254:255], %[ctl] offset:1024 sc0\n"
    "s_mov_b64 exec, -1\n"
    "s_mov_b64 s[40:41], %[kv]\n"
    "s_mov_b64 s[44:45], %[o]\n"
    "s_and_b32 s41, s41, 0xffff\n"
    "s_mov_b32 s43, 0x20000\n"
    "s_and_b32 s45, s45, 0xffff\n"
    "s_mov_b32 s46, 0x4000\n"
    "s_mov_b32 s47, 0x20000\n"
    "s_mov_b32 s64, 0x3e0293ee\n"
    "s_mov_b32 s65, 0x3e0293ee\n"
    "v_add_u32 v199, 0x1000, v198\n"
    "v_add_u32 v197, s60, v198\n"
    "s_lshl_b32 s56, s59, 13\n"
    "s_add_u32 s51, s56, s60\n"
    "s_mov_b64 s[38:39], %[kv]\n"
    "s_add_u32 s38, s38, s56\n"
    "s_addc_u32 s39, s39, 0\n"
    "s_lshl_b32 s50, s61, 15\n"
    "s_add_u32 s50, s50, s56\n"
    "v_add_u32 v200, s51, v198\n"
    "v_and_b32 v208, 10, v205\n"
    "v_bfe_u32 v209, v205, 2, 1\n"
    "v_or_b32 v208, v208, v209\n"
    "v_and_b32 v209, 1, v205\n"
    "v_lshl_or_b32 v208, v209, 2, v208\n"
    "v_lshlrev_b32 v209, 1, v206\n"
    "v_xor_b32 v208, v208, v209\n"
    "v_lshlrev_b32 v208, 4, v208\n"
    "v_lshl_add_u32 v208, v205, 9, v208\n"
    "v_and_b32 v209, 1, v206\n"
    "v_lshrrev_b32 v210, 1, v206\n"
    "v_lshl_or_b32 v209, v210, 3, v209\n"
    "v_lshlrev_b32 v210, 1, v205\n"
    "v_xor_b32 v209, v209, v210\n"
    "v_lshlrev_b32 v209, 4, v209\n"
    "v_lshl_add_u32 v209, v206, 11, v209\n"
    "s_bitcmp1_b32 s59, 0\n"
    "s_cselect_b64 vcc, -1, 0\n"
    "v_cndmask_b32 v201, v208, v209, vcc\n"
    "v_add_u32 v201, s79, v201\n"
    "v_lshrrev_b32 v144, 8, v198\n"
    "v_lshlrev_b32 v144, 4, v144\n"
    "v_xor_b32 v144, v144, v198\n"
    "v_xor_b32 v145, 0x40, v144\n"
    "v_xor_b32 v146, 0x80, v144\n"
    "v_xor_b32 v147, 0xc0, v144\n"
    "s_mov_b64 s[48:49], %[q]\n"
    "s_add_u32 m0, s51, 32768\n"
    "s_nop 0\n"
    "global_load_lds_dwordx4 v144, s[48:49] offset:0 nt\n"
    "s_add_u32 m0, s51, 32768\n"
    "s_nop 0\n"
    "global_load_lds_dwordx4 v145, s[48:49] offset:1024 nt\n"
    "s_add_u32 m0, s51, 32768\n"
    "s_nop 0\n"
    "global_load_lds_dwordx4 v146, s[48:49] offset:2048 nt\n"
    "s_add_u32 m0, s51, 32768\n"
    "s_nop 0\n"
    "global_load_lds_dwordx4 v147, s[48:49] offset:3072 nt\n"
    "s_add_u32 s48, s48, 0x1000\n"
    "s_addc_u32 s49, s49, 0\n"
    "s_add_u32 m0, s51, 36864\n"
    "s_nop 0\n"
    "global_load_lds_dwordx4 v144, s[48:49] offset:0 nt\n"
    "s_add_u32 m0, s51, 36864\n"
    "s_nop 0\n"
    "global_load_lds_dwordx4 v145, s[48:49] offset:1024 nt\n"
    "s_add_u32 m0, s51, 36864\n"
    "s_nop 0\n"
    "global_load_lds_dwordx4 v146, s[48:49] offset:2048 nt\n"
    "s_add_u32 m0, s51, 36864\n"
    "s_nop 0\n"
    "global_load_lds_dwordx4 v147, s[48:49] offset:3072 nt\n"
    "s_waitcnt vmcnt(25)\n"
    "v_lshlrev_b32 v0, 16, v64\n"
    "v_and_b32 v1, 0xffff0000, v64\n"
    "v_lshlrev_b32 v32, 16, v80\n"
    "v_and_b32 v33, 0xffff0000, v80\n"
    "v_lshlrev_b32 v2, 16, v65\n"
    "v_and_b32 v3, 0xffff0000, v65\n"
    "v_lshlrev_b32 v34, 16, v81\n"
    "v_and_b32 v35, 0xffff0000, v81\n"
    "v_lshlrev_b32 v4, 16, v66\n"
    "v_and_b32 v5, 0xffff0000, v66\n"
    "v_lshlrev_b32 v36, 16, v82\n"
    "v_and_b32 v37, 0xffff0000, v82\n"
    "v_lshlrev_b32 v6, 16, v67\n"
    "v_and_b32 v7, 0xffff0000, v67\n"
    "v_lshlrev_b32 v38, 16, v83\n"
    "v_and_b32 v39, 0xffff0000, v83\n"
    "v_lshlrev_b32 v8, 16, v68\n"
    "v_and_b32 v9, 0xffff0000, v68\n"
    "v_lshlrev_b32 v40, 16, v84\n"
    "v_and_b32 v41, 0xffff0000, v84\n"
    "v_lshlrev_b32 v10, 16, v69\n"
    "v_and_b32 v11, 0xffff0000, v69\n"
    "v_lshlrev_b32 v42, 16, v85\n"
    "v_and_b32 v43, 0xffff0000, v85\n"
    "v_lshlrev_b32 v12, 16, v70\n"
    "v_and_b32 v13, 0xffff0000, v70\n"
    "v_lshlrev_b32 v44, 16, v86\n"
    "v_and_b32 v45, 0xffff0000, v86\n"
    "v_lshlrev_b32 v14, 16, v71\n"
    "v_and_b32 v15, 0xffff0000, v71\n"
    "v_lshlrev_b32 v46, 16, v87\n"
    "v_and_b32 v47, 0xffff0000, v87\n"
    "v_lshlrev_b32 v16, 16, v72\n"
    "v_and_b32 v17, 0xffff0000, v72\n"
    "v_lshlrev_b32 v48, 16, v88\n"
    "v_and_b32 v49, 0xffff0000, v88\n"
    "v_lshlrev_b32 v18, 16, v73\n"
    "v_and_b32 v19, 0xffff0000, v73\n"
    "v_lshlrev_b32 v50, 16, v89\n"
    "v_and_b32 v51, 0xffff0000, v89\n"
    "v_lshlrev_b32 v20, 16, v74\n"
    "v_and_b32 v21, 0xffff0000, v74\n"
    "v_lshlrev_b32 v52, 16, v90\n"
    "v_and_b32 v53, 0xffff0000, v90\n"
    "v_lshlrev_b32 v22, 16, v75\n"
    "v_and_b32 v23, 0xffff0000, v75\n"
    "v_lshlrev_b32 v54, 16, v91\n"
    "v_and_b32 v55, 0xffff0000, v91\n"
    "v_lshlrev_b32 v24, 16, v76\n"
    "v_and_b32 v25, 0xffff0000, v76\n"
    "v_lshlrev_b32 v56, 16, v92\n"
    "v_and_b32 v57, 0xffff0000, v92\n"
    "v_lshlrev_b32 v26, 16, v77\n"
    "v_and_b32 v27, 0xffff0000, v77\n"
    "v_lshlrev_b32 v58, 16, v93\n"
    "v_and_b32 v59, 0xffff0000, v93\n"
    "v_lshlrev_b32 v28, 16, v78\n"
    "v_and_b32 v29, 0xffff0000, v78\n"
    "v_lshlrev_b32 v60, 16, v94\n"
    "v_and_b32 v61, 0xffff0000, v94\n"
    "v_lshlrev_b32 v30, 16, v79\n"
    "v_and_b32 v31, 0xffff0000, v79\n"
    "v_lshlrev_b32 v62, 16, v95\n"
    "v_and_b32 v63, 0xffff0000, v95\n"
    "v_xor_b32 v244, 0x0, v201\n"
    "v_xor_b32 v245, 0x10, v201\n"
    "v_xor_b32 v246, 0x80, v201\n"
    "v_xor_b32 v247, 0x90, v201\n"
    "s_waitcnt vmcnt(17)\n"
    "ds_read_b128 v[220:223], v244 offset:0\n"
    "ds_read_b128 v[224:227], v245 offset:0\n"
    "ds_read_b128 v[228:231], v246 offset:0\n"
    "ds_read_b128 v[232:235], v247 offset:0\n"
    "s_waitcnt lgkmcnt(2)\n"
    "v_cvt_f32_i32 v220, v220\n"
    "v_cvt_f32_i32 v221, v221\n"
    "v_cvt_f32_i32 v222, v222\n"
    "v_cvt_f32_i32 v223, v223\n"
    "v_cvt_f32_i32 v224, v224\n"
    "v_cvt_f32_i32 v225, v225\n"
    "v_cvt_f32_i32 v226, v226\n"
    "v_cvt_f32_i32 v227, v227\n"
    "v_cvt_pk_bf16_f32 v220, 0, v220\n"
    "v_cvt_pk_bf16_f32 v221, 0, v221\n"
    "v_cvt_pk_bf16_f32 v222, 0, v222\n"
    "v_cvt_pk_bf16_f32 v223, 0, v223\n"
    "v_cvt_pk_bf16_f32 v224, 0, v224\n"
    "v_cvt_pk_bf16_f32 v225, 0, v225\n"
    "v_cvt_pk_bf16_f32 v226, 0, v226\n"
    "v_cvt_pk_bf16_f32 v227, 0, v227\n"
    "v_pk_add_f32 v[220:221], v[220:221], v[32:33] neg_lo:[0,1] neg_hi:[0,1]\n"
    "v_pk_add_f32 v[222:223], v[222:223], v[34:35] neg_lo:[0,1] neg_hi:[0,1]\n"
    "v_pk_add_f32 v[224:225], v[224:225], v[36:37] neg_lo:[0,1] neg_hi:[0,1]\n"
    "v_pk_add_f32 v[226:227], v[226:227], v[38:39] neg_lo:[0,1] neg_hi:[0,1]\n"
    "v_cvt_pk_bf16_f32 v220, 0, v220\n"
    "v_cvt_pk_bf16_f32 v221, 0, v221\n"
    "v_cvt_pk_bf16_f32 v222, 0, v222\n"
    "v_cvt_pk_bf16_f32 v223, 0, v223\n"
    "v_cvt_pk_bf16_f32 v224, 0, v224\n"
    "v_cvt_pk_bf16_f32 v225, 0, v225\n"
    "v_cvt_pk_bf16_f32 v226, 0, v226\n"
    "v_cvt_pk_bf16_f32 v227, 0, v227\n"
    "v_pk_mul_f32 v[220:221], v[220:221], v[0:1]\n"
    "v_pk_mul_f32 v[222:223], v[222:223], v[2:3]\n"
    "v_pk_mul_f32 v[224:225], v[224:225], v[4:5]\n"
    "v_pk_mul_f32 v[226:227], v[226:227], v[6:7]\n"
    "v_cvt_pk_bf16_f32 v220, 0, v220\n"
    "v_cvt_pk_bf16_f32 v221, 0, v221\n"
    "v_cvt_pk_bf16_f32 v222, 0, v222\n"
    "v_cvt_pk_bf16_f32 v223, 0, v223\n"
    "v_cvt_pk_bf16_f32 v224, 0, v224\n"
    "v_cvt_pk_bf16_f32 v225, 0, v225\n"
    "v_cvt_pk_bf16_f32 v226, 0, v226\n"
    "v_cvt_pk_bf16_f32 v227, 0, v227\n"
    "v_pk_mul_f32 v[220:221], v[220:221], s[64:65]\n"
    "v_pk_mul_f32 v[222:223], v[222:223], s[64:65]\n"
    "v_pk_mul_f32 v[224:225], v[224:225], s[64:65]\n"
    "v_pk_mul_f32 v[226:227], v[226:227], s[64:65]\n"
    "v_cvt_pk_bf16_f32 v236, v220, v221\n"
    "v_cvt_pk_bf16_f32 v237, v222, v223\n"
    "v_cvt_pk_bf16_f32 v238, v224, v225\n"
    "v_cvt_pk_bf16_f32 v239, v226, v227\n"
    "ds_write_b128 v200, v[236:239] offset:0\n"
    "s_cmp_lt_u32 s61, 6\n"
    "s_cbranch_scc1 .Lhe0\n"
    "s_add_u32 s48, s48, 0x1000\n"
    "s_addc_u32 s49, s49, 0\n"
    "s_add_u32 m0, s51, 65536\n"
    "s_nop 0\n"
    "global_load_lds_dwordx4 v144, s[48:49] offset:0 nt\n"
    "s_add_u32 m0, s51, 65536\n"
    "s_nop 0\n"
    "global_load_lds_dwordx4 v145, s[48:49] offset:1024 nt\n"
    "v_accvgpr_write_b32 a192, v236\n"
    "v_accvgpr_write_b32 a193, v237\n"
    "v_accvgpr_write_b32 a194, v238\n"
    "v_accvgpr_write_b32 a195, v239\n"
    "s_branch .Lhj0\n"
    ".Lhe0:\n"
    "buffer_store_dwordx4 v[236:239], v198, s[40:43], s50 offen offset:0 sc1\n"
    ".Lhj0:\n"
    "ds_read_b128 v[220:223], v244 offset:256\n"
    "ds_read_b128 v[224:227], v245 offset:256\n"
    "s_waitcnt lgkmcnt(2)\n"
    "v_cvt_f32_i32 v228, v228\n"
    "v_cvt_f32_i32 v229, v229\n"
    "v_cvt_f32_i32 v230, v230\n"
    "v_cvt_f32_i32 v231, v231\n"
    "v_cvt_f32_i32 v232, v232\n"
    "v_cvt_f32_i32 v233, v233\n"
    "v_cvt_f32_i32 v234, v234\n"
    "v_cvt_f32_i32 v235, v235\n"
    "v_cvt_pk_bf16_f32 v228, 0, v228\n"
    "v_cvt_pk_bf16_f32 v229, 0, v229\n"
    "v_cvt_pk_bf16_f32 v230, 0, v230\n"
    "v_cvt_pk_bf16_f32 v231, 0, v231\n"
    "v_cvt_pk_bf16_f32 v232, 0, v232\n"
    "v_cvt_pk_bf16_f32 v233, 0, v233\n"
    "v_cvt_pk_bf16_f32 v234, 0, v234\n"
    "v_cvt_pk_bf16_f32 v235, 0, v235\n"
    "v_pk_add_f32 v[228:229], v[228:229], v[40:41] neg_lo:[0,1] neg_hi:[0,1]\n"
    "v_pk_add_f32 v[230:231], v[230:231], v[42:43] neg_lo:[0,1] neg_hi:[0,1]\n"
    "v_pk_add_f32 v[232:233], v[232:233], v[44:45] neg_lo:[0,1] neg_hi:[0,1]\n"
    "v_pk_add_f32 v[234:235], v[234:235], v[46:47] neg_lo:[0,1] neg_hi:[0,1]\n"
    "v_cvt_pk_bf16_f32 v228, 0, v228\n"
    "v_cvt_pk_bf16_f32 v229, 0, v229\n"
    "v_cvt_pk_bf16_f32 v230, 0, v230\n"
    "v_cvt_pk_bf16_f32 v231, 0, v231\n"
    "v_cvt_pk_bf16_f32 v232, 0, v232\n"
    "v_cvt_pk_bf16_f32 v233, 0, v233\n"
    "v_cvt_pk_bf16_f32 v234, 0, v234\n"
    "v_cvt_pk_bf16_f32 v235, 0, v235\n"
    "v_pk_mul_f32 v[228:229], v[228:229], v[8:9]\n"
    "v_pk_mul_f32 v[230:231], v[230:231], v[10:11]\n"
    "v_pk_mul_f32 v[232:233], v[232:233], v[12:13]\n"
    "v_pk_mul_f32 v[234:235], v[234:235], v[14:15]\n"
    "v_cvt_pk_bf16_f32 v228, 0, v228\n"
    "v_cvt_pk_bf16_f32 v229, 0, v229\n"
    "v_cvt_pk_bf16_f32 v230, 0, v230\n"
    "v_cvt_pk_bf16_f32 v231, 0, v231\n"
    "v_cvt_pk_bf16_f32 v232, 0, v232\n"
    "v_cvt_pk_bf16_f32 v233, 0, v233\n"
    "v_cvt_pk_bf16_f32 v234, 0, v234\n"
    "v_cvt_pk_bf16_f32 v235, 0, v235\n"
    "v_pk_mul_f32 v[228:229], v[228:229], s[64:65]\n"
    "v_pk_mul_f32 v[230:231], v[230:231], s[64:65]\n"
    "v_pk_mul_f32 v[232:233], v[232:233], s[64:65]\n"
    "v_pk_mul_f32 v[234:235], v[234:235], s[64:65]\n"
    "v_cvt_pk_bf16_f32 v240, v228, v229\n"
    "v_cvt_pk_bf16_f32 v241, v230, v231\n"
    "v_cvt_pk_bf16_f32 v242, v232, v233\n"
    "v_cvt_pk_bf16_f32 v243, v234, v235\n"
    "ds_write_b128 v200, v[240:243] offset:1024\n"
    "s_cmp_lt_u32 s61, 6\n"
    "s_cbranch_scc1 .Lhe1\n"
    "s_add_u32 m0, s51, 65536\n"
    "s_nop 0\n"
    "global_load_lds_dwordx4 v146, s[48:49] offset:2048 nt\n"
    "s_add_u32 m0, s51, 65536\n"
    "s_nop 0\n"
    "global_load_lds_dwordx4 v147, s[48:49] offset:3072 nt\n"
    "s_add_u32 s48, s48, 0x1000\n"
    "s_addc_u32 s49, s49, 0\n"
    "s_add_u32 m0, s51, 69632\n"
    "s_nop 0\n"
    "global_load_lds_dwordx4 v144, s[48:49] offset:0 nt\n"
    "v_accvgpr_write_b32 a196, v240\n"
    "v_accvgpr_write_b32 a197, v241\n"
    "v_accvgpr_write_b32 a198, v242\n"
    "v_accvgpr_write_b32 a199, v243\n"
    "s_branch .Lhj1\n"
    ".Lhe1:\n"
    "buffer_store_dwordx4 v[240:243], v198, s[40:43], s50 offen offset:1024 sc1\n"
    ".Lhj1:\n"
    "ds_read_b128 v[228:231], v246 offset:256\n"
    "ds_read_b128 v[232:235], v247 offset:256\n"
    "s_waitcnt lgkmcnt(2)\n"
    "v_cvt_f32_i32 v220, v220\n"
    "v_cvt_f32_i32 v221, v221\n"
    "v_cvt_f32_i32 v222, v222\n"
    "v_cvt_f32_i32 v223, v223\n"
    "v_cvt_f32_i32 v224, v224\n"
    "v_cvt_f32_i32 v225, v225\n"
    "v_cvt_f32_i32 v226, v226\n"
    "v_cvt_f32_i32 v227, v227\n"
    "v_cvt_pk_bf16_f32 v220, 0, v220\n"
    "v_cvt_pk_bf16_f32 v221, 0, v221\n"
    "v_cvt_pk_bf16_f32 v222, 0, v222\n"
    "v_cvt_pk_bf16_f32 v223, 0, v223\n"
    "v_cvt_pk_bf16_f32 v224, 0, v224\n"
    "v_cvt_pk_bf16_f32 v225, 0, v225\n"
    "v_cvt_pk_bf16_f32 v226, 0, v226\n"
    "v_cvt_pk_bf16_f32 v227, 0, v227\n"
    "v_pk_add_f32 v[220:221], v[220:221], v[48:49] neg_lo:[0,1] neg_hi:[0,1]\n"
    "v_pk_add_f32 v[222:223], v[222:223], v[50:51] neg_lo:[0,1] neg_hi:[0,1]\n"
    "v_pk_add_f32 v[224:225], v[224:225], v[52:53] neg_lo:[0,1] neg_hi:[0,1]\n"
    "v_pk_add_f32 v[226:227], v[226:227], v[54:55] neg_lo:[0,1] neg_hi:[0,1]\n"
    "v_cvt_pk_bf16_f32 v220, 0, v220\n"
    "v_cvt_pk_bf16_f32 v221, 0, v221\n"
    "v_cvt_pk_bf16_f32 v222, 0, v222\n"
    "v_cvt_pk_bf16_f32 v223, 0, v223\n"
    "v_cvt_pk_bf16_f32 v224, 0, v224\n"
    "v_cvt_pk_bf16_f32 v225, 0, v225\n"
    "v_cvt_pk_bf16_f32 v226, 0, v226\n"
    "v_cvt_pk_bf16_f32 v227, 0, v227\n"
    "v_pk_mul_f32 v[220:221], v[220:221], v[16:17]\n"
    "v_pk_mul_f32 v[222:223], v[222:223], v[18:19]\n"
    "v_pk_mul_f32 v[224:225], v[224:225], v[20:21]\n"
    "v_pk_mul_f32 v[226:227], v[226:227], v[22:23]\n"
    "v_cvt_pk_bf16_f32 v220, 0, v220\n"
    "v_cvt_pk_bf16_f32 v221, 0, v221\n"
    "v_cvt_pk_bf16_f32 v222, 0, v222\n"
    "v_cvt_pk_bf16_f32 v223, 0, v223\n"
    "v_cvt_pk_bf16_f32 v224, 0, v224\n"
    "v_cvt_pk_bf16_f32 v225, 0, v225\n"
    "v_cvt_pk_bf16_f32 v226, 0, v226\n"
    "v_cvt_pk_bf16_f32 v227, 0, v227\n"
    "v_pk_mul_f32 v[220:221], v[220:221], s[64:65]\n"
    "v_pk_mul_f32 v[222:223], v[222:223], s[64:65]\n"
    "v_pk_mul_f32 v[224:225], v[224:225], s[64:65]\n"
    "v_pk_mul_f32 v[226:227], v[226:227], s[64:65]\n"
    "v_cvt_pk_bf16_f32 v204, v220, v221\n"
    "v_cvt_pk_bf16_f32 v205, v222, v223\n"
    "v_cvt_pk_bf16_f32 v206, v224, v225\n"
    "v_cvt_pk_bf16_f32 v207, v226, v227\n"
    "ds_write_b128 v200, v[204:207] offset:2048\n"
    "s_cmp_lt_u32 s61, 6\n"
    "s_cbranch_scc1 .Lhe2\n"
    "s_add_u32 m0, s51, 69632\n"
    "s_nop 0\n"
    "global_load_lds_dwordx4 v145, s[48:49] offset:1024 nt\n"
    "s_add_u32 m0, s51, 69632\n"
    "s_nop 0\n"
    "global_load_lds_dwordx4 v146, s[48:49] offset:2048 nt\n"
    "s_add_u32 m0, s51, 69632\n"
    "s_nop 0\n"
    "global_load_lds_dwordx4 v147, s[48:49] offset:3072 nt\n"
    "v_accvgpr_write_b32 a200, v204\n"
    "v_accvgpr_write_b32 a201, v205\n"
    "v_accvgpr_write_b32 a202, v206\n"
    "v_accvgpr_write_b32 a203, v207\n"
    "s_waitcnt vmcnt(17)\n"
    "s_branch .Lhj2\n"
    ".Lhe2:\n"
    "buffer_store_dwordx4 v[204:207], v198, s[40:43], s50 offen offset:2048 sc1\n"
    "s_waitcnt vmcnt(12)\n"
    ".Lhj2:\n"
    "ds_read_b128 v[220:223], v244 offset:8192\n"
    "ds_read_b128 v[224:227], v245 offset:8192\n"
    "s_waitcnt lgkmcnt(2)\n"
    "v_cvt_f32_i32 v228, v228\n"
    "v_cvt_f32_i32 v229, v229\n"
    "v_cvt_f32_i32 v230, v230\n"
    "v_cvt_f32_i32 v231, v231\n"
    "v_cvt_f32_i32 v232, v232\n"
    "v_cvt_f32_i32 v233, v233\n"
    "v_cvt_f32_i32 v234, v234\n"
    "v_cvt_f32_i32 v235, v235\n"
    "v_cvt_pk_bf16_f32 v228, 0, v228\n"
    "v_cvt_pk_bf16_f32 v229, 0, v229\n"
    "v_cvt_pk_bf16_f32 v230, 0, v230\n"
    "v_cvt_pk_bf16_f32 v231, 0, v231\n"
    "v_cvt_pk_bf16_f32 v232, 0, v232\n"
    "v_cvt_pk_bf16_f32 v233, 0, v233\n"
    "v_cvt_pk_bf16_f32 v234, 0, v234\n"
    "v_cvt_pk_bf16_f32 v235, 0, v235\n"
    "v_pk_add_f32 v[228:229], v[228:229], v[56:57] neg_lo:[0,1] neg_hi:[0,1]\n"
    "v_pk_add_f32 v[230:231], v[230:231], v[58:59] neg_lo:[0,1] neg_hi:[0,1]\n"
    "v_pk_add_f32 v[232:233], v[232:233], v[60:61] neg_lo:[0,1] neg_hi:[0,1]\n"
    "v_pk_add_f32 v[234:235], v[234:235], v[62:63] neg_lo:[0,1] neg_hi:[0,1]\n"
    "v_cvt_pk_bf16_f32 v228, 0, v228\n"
    "v_cvt_pk_bf16_f32 v229, 0, v229\n"
    "v_cvt_pk_bf16_f32 v230, 0, v230\n"
    "v_cvt_pk_bf16_f32 v231, 0, v231\n"
    "v_cvt_pk_bf16_f32 v232, 0, v232\n"
    "v_cvt_pk_bf16_f32 v233, 0, v233\n"
    "v_cvt_pk_bf16_f32 v234, 0, v234\n"
    "v_cvt_pk_bf16_f32 v235, 0, v235\n"
    "v_pk_mul_f32 v[228:229], v[228:229], v[24:25]\n"
    "v_pk_mul_f32 v[230:231], v[230:231], v[26:27]\n"
    "v_pk_mul_f32 v[232:233], v[232:233], v[28:29]\n"
    "v_pk_mul_f32 v[234:235], v[234:235], v[30:31]\n"
    "v_cvt_pk_bf16_f32 v228, 0, v228\n"
    "v_cvt_pk_bf16_f32 v229, 0, v229\n"
    "v_cvt_pk_bf16_f32 v230, 0, v230\n"
    "v_cvt_pk_bf16_f32 v231, 0, v231\n"
    "v_cvt_pk_bf16_f32 v232, 0, v232\n"
    "v_cvt_pk_bf16_f32 v233, 0, v233\n"
    "v_cvt_pk_bf16_f32 v234, 0, v234\n"
    "v_cvt_pk_bf16_f32 v235, 0, v235\n"
    "v_pk_mul_f32 v[228:229], v[228:229], s[64:65]\n"
    "v_pk_mul_f32 v[230:231], v[230:231], s[64:65]\n"
    "v_pk_mul_f32 v[232:233], v[232:233], s[64:65]\n"
    "v_pk_mul_f32 v[234:235], v[234:235], s[64:65]\n"
    "v_cvt_pk_bf16_f32 v208, v228, v229\n"
    "v_cvt_pk_bf16_f32 v209, v230, v231\n"
    "v_cvt_pk_bf16_f32 v210, v232, v233\n"
    "v_cvt_pk_bf16_f32 v211, v234, v235\n"
    "ds_write_b128 v200, v[208:211] offset:3072\n"
    "s_cmp_lt_u32 s61, 6\n"
    "s_cbranch_scc1 .Lhe3\n"
    "v_accvgpr_write_b32 a204, v208\n"
    "v_accvgpr_write_b32 a205, v209\n"
    "v_accvgpr_write_b32 a206, v210\n"
    "v_accvgpr_write_b32 a207, v211\n"
    "s_branch .Lhj3\n"
    ".Lhe3:\n"
    "buffer_store_dwordx4 v[208:211], v198, s[40:43], s50 offen offset:3072 sc1\n"
    ".Lhj3:\n"
    "ds_read_b128 v[228:231], v246 offset:8192\n"
    "ds_read_b128 v[232:235], v247 offset:8192\n"
    "s_waitcnt lgkmcnt(2)\n"
    "v_cvt_f32_i32 v220, v220\n"
    "v_cvt_f32_i32 v221, v221\n"
    "v_cvt_f32_i32 v222, v222\n"
    "v_cvt_f32_i32 v223, v223\n"
    "v_cvt_f32_i32 v224, v224\n"
    "v_cvt_f32_i32 v225, v225\n"
    "v_cvt_f32_i32 v226, v226\n"
    "v_cvt_f32_i32 v227, v227\n"
    "v_cvt_pk_bf16_f32 v220, 0, v220\n"
    "v_cvt_pk_bf16_f32 v221, 0, v221\n"
    "v_cvt_pk_bf16_f32 v222, 0, v222\n"
    "v_cvt_pk_bf16_f32 v223, 0, v223\n"
    "v_cvt_pk_bf16_f32 v224, 0, v224\n"
    "v_cvt_pk_bf16_f32 v225, 0, v225\n"
    "v_cvt_pk_bf16_f32 v226, 0, v226\n"
    "v_cvt_pk_bf16_f32 v227, 0, v227\n"
    "v_pk_add_f32 v[220:221], v[220:221], v[32:33] neg_lo:[0,1] neg_hi:[0,1]\n"
    "v_pk_add_f32 v[222:223], v[222:223], v[34:35] neg_lo:[0,1] neg_hi:[0,1]\n"
    "v_pk_add_f32 v[224:225], v[224:225], v[36:37] neg_lo:[0,1] neg_hi:[0,1]\n"
    "v_pk_add_f32 v[226:227], v[226:227], v[38:39] neg_lo:[0,1] neg_hi:[0,1]\n"
    "v_cvt_pk_bf16_f32 v220, 0, v220\n"
    "v_cvt_pk_bf16_f32 v221, 0, v221\n"
    "v_cvt_pk_bf16_f32 v222, 0, v222\n"
    "v_cvt_pk_bf16_f32 v223, 0, v223\n"
    "v_cvt_pk_bf16_f32 v224, 0, v224\n"
    "v_cvt_pk_bf16_f32 v225, 0, v225\n"
    "v_cvt_pk_bf16_f32 v226, 0, v226\n"
    "v_cvt_pk_bf16_f32 v227, 0, v227\n"
    "v_pk_mul_f32 v[220:221], v[220:221], v[0:1]\n"
    "v_pk_mul_f32 v[222:223], v[222:223], v[2:3]\n"
    "v_pk_mul_f32 v[224:225], v[224:225], v[4:5]\n"
    "v_pk_mul_f32 v[226:227], v[226:227], v[6:7]\n"
    "v_cvt_pk_bf16_f32 v220, 0, v220\n"
    "v_cvt_pk_bf16_f32 v221, 0, v221\n"
    "v_cvt_pk_bf16_f32 v222, 0, v222\n"
    "v_cvt_pk_bf16_f32 v223, 0, v223\n"
    "v_cvt_pk_bf16_f32 v224, 0, v224\n"
    "v_cvt_pk_bf16_f32 v225, 0, v225\n"
    "v_cvt_pk_bf16_f32 v226, 0, v226\n"
    "v_cvt_pk_bf16_f32 v227, 0, v227\n"
    "v_pk_mul_f32 v[220:221], v[220:221], s[64:65]\n"
    "v_pk_mul_f32 v[222:223], v[222:223], s[64:65]\n"
    "v_pk_mul_f32 v[224:225], v[224:225], s[64:65]\n"
    "v_pk_mul_f32 v[226:227], v[226:227], s[64:65]\n"
    "v_cvt_pk_bf16_f32 v236, v220, v221\n"
    "v_cvt_pk_bf16_f32 v237, v222, v223\n"
    "v_cvt_pk_bf16_f32 v238, v224, v225\n"
    "v_cvt_pk_bf16_f32 v239, v226, v227\n"
    "ds_write_b128 v200, v[236:239] offset:4096\n"
    "s_cmp_lt_u32 s61, 6\n"
    "s_cbranch_scc1 .Lhe4\n"
    "v_accvgpr_write_b32 a208, v236\n"
    "v_accvgpr_write_b32 a209, v237\n"
    "v_accvgpr_write_b32 a210, v238\n"
    "v_accvgpr_write_b32 a211, v239\n"
    "s_branch .Lhj4\n"
    ".Lhe4:\n"
    "buffer_store_dwordx4 v[236:239], v199, s[40:43], s50 offen offset:0 sc1\n"
    ".Lhj4:\n"
    "ds_read_b128 v[220:223], v244 offset:8448\n"
    "ds_read_b128 v[224:227], v245 offset:8448\n"
    "s_waitcnt lgkmcnt(2)\n"
    "v_cvt_f32_i32 v228, v228\n"
    "v_cvt_f32_i32 v229, v229\n"
    "v_cvt_f32_i32 v230, v230\n"
    "v_cvt_f32_i32 v231, v231\n"
    "v_cvt_f32_i32 v232, v232\n"
    "v_cvt_f32_i32 v233, v233\n"
    "v_cvt_f32_i32 v234, v234\n"
    "v_cvt_f32_i32 v235, v235\n"
    "v_cvt_pk_bf16_f32 v228, 0, v228\n"
    "v_cvt_pk_bf16_f32 v229, 0, v229\n"
    "v_cvt_pk_bf16_f32 v230, 0, v230\n"
    "v_cvt_pk_bf16_f32 v231, 0, v231\n"
    "v_cvt_pk_bf16_f32 v232, 0, v232\n"
    "v_cvt_pk_bf16_f32 v233, 0, v233\n"
    "v_cvt_pk_bf16_f32 v234, 0, v234\n"
    "v_cvt_pk_bf16_f32 v235, 0, v235\n"
    "v_pk_add_f32 v[228:229], v[228:229], v[40:41] neg_lo:[0,1] neg_hi:[0,1]\n"
    "v_pk_add_f32 v[230:231], v[230:231], v[42:43] neg_lo:[0,1] neg_hi:[0,1]\n"
    "v_pk_add_f32 v[232:233], v[232:233], v[44:45] neg_lo:[0,1] neg_hi:[0,1]\n"
    "v_pk_add_f32 v[234:235], v[234:235], v[46:47] neg_lo:[0,1] neg_hi:[0,1]\n"
    "v_cvt_pk_bf16_f32 v228, 0, v228\n"
    "v_cvt_pk_bf16_f32 v229, 0, v229\n"
    "v_cvt_pk_bf16_f32 v230, 0, v230\n"
    "v_cvt_pk_bf16_f32 v231, 0, v231\n"
    "v_cvt_pk_bf16_f32 v232, 0, v232\n"
    "v_cvt_pk_bf16_f32 v233, 0, v233\n"
    "v_cvt_pk_bf16_f32 v234, 0, v234\n"
    "v_cvt_pk_bf16_f32 v235, 0, v235\n"
    "v_pk_mul_f32 v[228:229], v[228:229], v[8:9]\n"
    "v_pk_mul_f32 v[230:231], v[230:231], v[10:11]\n"
    "v_pk_mul_f32 v[232:233], v[232:233], v[12:13]\n"
    "v_pk_mul_f32 v[234:235], v[234:235], v[14:15]\n"
    "v_cvt_pk_bf16_f32 v228, 0, v228\n"
    "v_cvt_pk_bf16_f32 v229, 0, v229\n"
    "v_cvt_pk_bf16_f32 v230, 0, v230\n"
    "v_cvt_pk_bf16_f32 v231, 0, v231\n"
    "v_cvt_pk_bf16_f32 v232, 0, v232\n"
    "v_cvt_pk_bf16_f32 v233, 0, v233\n"
    "v_cvt_pk_bf16_f32 v234, 0, v234\n"
    "v_cvt_pk_bf16_f32 v235, 0, v235\n"
    "v_pk_mul_f32 v[228:229], v[228:229], s[64:65]\n"
    "v_pk_mul_f32 v[230:231], v[230:231], s[64:65]\n"
    "v_pk_mul_f32 v[232:233], v[232:233], s[64:65]\n"
    "v_pk_mul_f32 v[234:235], v[234:235], s[64:65]\n"
    "v_cvt_pk_bf16_f32 v240, v228, v229\n"
    "v_cvt_pk_bf16_f32 v241, v230, v231\n"
    "v_cvt_pk_bf16_f32 v242, v232, v233\n"
    "v_cvt_pk_bf16_f32 v243, v234, v235\n"
    "ds_write_b128 v200, v[240:243] offset:5120\n"
    "s_cmp_lt_u32 s61, 6\n"
    "s_cbranch_scc1 .Lhe5\n"
    "v_accvgpr_write_b32 a212, v240\n"
    "v_accvgpr_write_b32 a213, v241\n"
    "v_accvgpr_write_b32 a214, v242\n"
    "v_accvgpr_write_b32 a215, v243\n"
    "s_branch .Lhj5\n"
    ".Lhe5:\n"
    "buffer_store_dwordx4 v[240:243], v199, s[40:43], s50 offen offset:1024 sc1\n"
    ".Lhj5:\n"
    "ds_read_b128 v[228:231], v246 offset:8448\n"
    "ds_read_b128 v[232:235], v247 offset:8448\n"
    "s_waitcnt lgkmcnt(2)\n"
    "v_cvt_f32_i32 v220, v220\n"
    "v_cvt_f32_i32 v221, v221\n"
    "v_cvt_f32_i32 v222, v222\n"
    "v_cvt_f32_i32 v223, v223\n"
    "v_cvt_f32_i32 v224, v224\n"
    "v_cvt_f32_i32 v225, v225\n"
    "v_cvt_f32_i32 v226, v226\n"
    "v_cvt_f32_i32 v227, v227\n"
    "v_cvt_pk_bf16_f32 v220, 0, v220\n"
    "v_cvt_pk_bf16_f32 v221, 0, v221\n"
    "v_cvt_pk_bf16_f32 v222, 0, v222\n"
    "v_cvt_pk_bf16_f32 v223, 0, v223\n"
    "v_cvt_pk_bf16_f32 v224, 0, v224\n"
    "v_cvt_pk_bf16_f32 v225, 0, v225\n"
    "v_cvt_pk_bf16_f32 v226, 0, v226\n"
    "v_cvt_pk_bf16_f32 v227, 0, v227\n"
    "v_pk_add_f32 v[220:221], v[220:221], v[48:49] neg_lo:[0,1] neg_hi:[0,1]\n"
    "v_pk_add_f32 v[222:223], v[222:223], v[50:51] neg_lo:[0,1] neg_hi:[0,1]\n"
    "v_pk_add_f32 v[224:225], v[224:225], v[52:53] neg_lo:[0,1] neg_hi:[0,1]\n"
    "v_pk_add_f32 v[226:227], v[226:227], v[54:55] neg_lo:[0,1] neg_hi:[0,1]\n"
    "v_cvt_pk_bf16_f32 v220, 0, v220\n"
    "v_cvt_pk_bf16_f32 v221, 0, v221\n"
    "v_cvt_pk_bf16_f32 v222, 0, v222\n"
    "v_cvt_pk_bf16_f32 v223, 0, v223\n"
    "v_cvt_pk_bf16_f32 v224, 0, v224\n"
    "v_cvt_pk_bf16_f32 v225, 0, v225\n"
    "v_cvt_pk_bf16_f32 v226, 0, v226\n"
    "v_cvt_pk_bf16_f32 v227, 0, v227\n"
    "v_pk_mul_f32 v[220:221], v[220:221], v[16:17]\n"
    "v_pk_mul_f32 v[222:223], v[222:223], v[18:19]\n"
    "v_pk_mul_f32 v[224:225], v[224:225], v[20:21]\n"
    "v_pk_mul_f32 v[226:227], v[226:227], v[22:23]\n"
    "v_cvt_pk_bf16_f32 v220, 0, v220\n"
    "v_cvt_pk_bf16_f32 v221, 0, v221\n"
    "v_cvt_pk_bf16_f32 v222, 0, v222\n"
    "v_cvt_pk_bf16_f32 v223, 0, v223\n"
    "v_cvt_pk_bf16_f32 v224, 0, v224\n"
    "v_cvt_pk_bf16_f32 v225, 0, v225\n"
    "v_cvt_pk_bf16_f32 v226, 0, v226\n"
    "v_cvt_pk_bf16_f32 v227, 0, v227\n"
    "v_pk_mul_f32 v[220:221], v[220:221], s[64:65]\n"
    "v_pk_mul_f32 v[222:223], v[222:223], s[64:65]\n"
    "v_pk_mul_f32 v[224:225], v[224:225], s[64:65]\n"
    "v_pk_mul_f32 v[226:227], v[226:227], s[64:65]\n"
    "v_cvt_pk_bf16_f32 v204, v220, v221\n"
    "v_cvt_pk_bf16_f32 v205, v222, v223\n"
    "v_cvt_pk_bf16_f32 v206, v224, v225\n"
    "v_cvt_pk_bf16_f32 v207, v226, v227\n"
    "ds_write_b128 v200, v[204:207] offset:6144\n"
    "s_cmp_lt_u32 s61, 6\n"
    "s_cbranch_scc1 .Lhe6\n"
    "v_accvgpr_write_b32 a216, v204\n"
    "v_accvgpr_write_b32 a217, v205\n"
    "v_accvgpr_write_b32 a218, v206\n"
    "v_accvgpr_write_b32 a219, v207\n"
    "s_branch .Lhj6\n"
    ".Lhe6:\n"
    "buffer_store_dwordx4 v[204:207], v199, s[40:43], s50 offen offset:2048 sc1\n"
    ".Lhj6:\n"
    "s_waitcnt lgkmcnt(0)\n"
    "v_cvt_f32_i32 v228, v228\n"
    "v_cvt_f32_i32 v229, v229\n"
    "v_cvt_f32_i32 v230, v230\n"
    "v_cvt_f32_i32 v231, v231\n"
    "v_cvt_f32_i32 v232, v232\n"
    "v_cvt_f32_i32 v233, v233\n"
    "v_cvt_f32_i32 v234, v234\n"
    "v_cvt_f32_i32 v235, v235\n"
    "v_cvt_pk_bf16_f32 v228, 0, v228\n"
    "v_cvt_pk_bf16_f32 v229, 0, v229\n"
    "v_cvt_pk_bf16_f32 v230, 0, v230\n"
    "v_cvt_pk_bf16_f32 v231, 0, v231\n"
    "v_cvt_pk_bf16_f32 v232, 0, v232\n"
    "v_cvt_pk_bf16_f32 v233, 0, v233\n"
    "v_cvt_pk_bf16_f32 v234, 0, v234\n"
    "v_cvt_pk_bf16_f32 v235, 0, v235\n"
    "v_pk_add_f32 v[228:229], v[228:229], v[56:57] neg_lo:[0,1] neg_hi:[0,1]\n"
    "v_pk_add_f32 v[230:231], v[230:231], v[58:59] neg_lo:[0,1] neg_hi:[0,1]\n"
    "v_pk_add_f32 v[232:233], v[232:233], v[60:61] neg_lo:[0,1] neg_hi:[0,1]\n"
    "v_pk_add_f32 v[234:235], v[234:235], v[62:63] neg_lo:[0,1] neg_hi:[0,1]\n"
    "v_cvt_pk_bf16_f32 v228, 0, v228\n"
    "v_cvt_pk_bf16_f32 v229, 0, v229\n"
    "v_cvt_pk_bf16_f32 v230, 0, v230\n"
    "v_cvt_pk_bf16_f32 v231, 0, v231\n"
    "v_cvt_pk_bf16_f32 v232, 0, v232\n"
    "v_cvt_pk_bf16_f32 v233, 0, v233\n"
    "v_cvt_pk_bf16_f32 v234, 0, v234\n"
    "v_cvt_pk_bf16_f32 v235, 0, v235\n"
    "v_pk_mul_f32 v[228:229], v[228:229], v[24:25]\n"
    "v_pk_mul_f32 v[230:231], v[230:231], v[26:27]\n"
    "v_pk_mul_f32 v[232:233], v[232:233], v[28:29]\n"
    "v_pk_mul_f32 v[234:235], v[234:235], v[30:31]\n"
    "v_cvt_pk_bf16_f32 v228, 0, v228\n"
    "v_cvt_pk_bf16_f32 v229, 0, v229\n"
    "v_cvt_pk_bf16_f32 v230, 0, v230\n"
    "v_cvt_pk_bf16_f32 v231, 0, v231\n"
    "v_cvt_pk_bf16_f32 v232, 0, v232\n"
    "v_cvt_pk_bf16_f32 v233, 0, v233\n"
    "v_cvt_pk_bf16_f32 v234, 0, v234\n"
    "v_cvt_pk_bf16_f32 v235, 0, v235\n"
    "v_pk_mul_f32 v[228:229], v[228:229], s[64:65]\n"
    "v_pk_mul_f32 v[230:231], v[230:231], s[64:65]\n"
    "v_pk_mul_f32 v[232:233], v[232:233], s[64:65]\n"
    "v_pk_mul_f32 v[234:235], v[234:235], s[64:65]\n"
    "v_cvt_pk_bf16_f32 v208, v228, v229\n"
    "v_cvt_pk_bf16_f32 v209, v230, v231\n"
    "v_cvt_pk_bf16_f32 v210, v232, v233\n"
    "v_cvt_pk_bf16_f32 v211, v234, v235\n"
    "ds_write_b128 v200, v[208:211] offset:7168\n"
    "s_cmp_lt_u32 s61, 6\n"
    "s_cbranch_scc1 .Lhe7\n"
    "v_accvgpr_write_b32 a220, v208\n"
    "v_accvgpr_write_b32 a221, v209\n"
    "v_accvgpr_write_b32 a222, v210\n"
    "v_accvgpr_write_b32 a223, v211\n"
    "s_branch .Lhj7\n"
    ".Lhe7:\n"
    "buffer_store_dwordx4 v[208:211], v199, s[40:43], s50 offen offset:3072 sc1\n"
    ".Lhj7:\n"
    "s_branch .Lrole_done\n"
    ".Lrole_v:\n"
    "global_load_dwordx2 v[64:65], v251, s[74:75] offset:0\n"
    "global_load_dwordx2 v[66:67], v251, s[74:75] offset:32\n"
    "global_load_dwordx2 v[68:69], v251, s[76:77] offset:0\n"
    "global_load_dwordx2 v[70:71], v251, s[76:77] offset:32\n"
    "global_load_dwordx2 v[72:73], v251, s[74:75] offset:0\n"
    "global_load_dwordx2 v[74:75], v251, s[74:75] offset:32\n"
    "global_load_dwordx2 v[72:73], v251, s[76:77] offset:0\n"
    "global_load_dwordx2 v[74:75], v251, s[76:77] offset:32\n"
    "s_lshl_b32 s56, s61, 15\n"
    "s_add_u32 s80, s72, s56\n"
    "s_addc_u32 s81, s73, 0\n"
    "s_mov_b32 m0, s79\n"
    "v_xor_b32 v248, 0x0, v250\n"
    "global_load_lds_dwordx4 v248, s[80:81] offset:0 nt\n"
    "v_xor_b32 v249, 0x20, v250\n"
    "global_load_lds_dwordx4 v249, s[80:81] offset:1024 nt\n"
    "v_xor_b32 v248, 0x10, v250\n"
    "global_load_lds_dwordx4 v248, s[80:81] offset:2048 nt\n"
    "v_xor_b32 v249, 0x30, v250\n"
    "global_load_lds_dwordx4 v249, s[80:81] offset:3072 nt\n"
    "s_add_u32 s80, s80, 0x1000\n"
    "s_addc_u32 s81, s81, 0\n"
    "s_add_u32 m0, m0, 0x1000\n"
    "v_xor_b32 v248, 0x80, v250\n"
    "global_load_lds_dwordx4 v248, s[80:81] offset:0 nt\n"
    "v_xor_b32 v249, 0xa0, v250\n"
    "global_load_lds_dwordx4 v249, s[80:81] offset:1024 nt\n"
    "v_xor_b32 v248, 0x90, v250\n"
    "global_load_lds_dwordx4 v248, s[80:81] offset:2048 nt\n"
    "v_xor_b32 v249, 0xb0, v250\n"
    "global_load_lds_dwordx4 v249, s[80:81] offset:3072 nt\n"
    "s_add_u32 s80, s80, 0x1000\n"
    "s_addc_u32 s81, s81, 0\n"
    "s_add_u32 m0, m0, 0x1000\n"
    "v_xor_b32 v248, 0x0, v250\n"
    "global_load_lds_dwordx4 v248, s[80:81] offset:0 nt\n"
    "v_xor_b32 v249, 0x20, v250\n"
    "global_load_lds_dwordx4 v249, s[80:81] offset:1024 nt\n"
    "v_xor_b32 v248, 0x10, v250\n"
    "global_load_lds_dwordx4 v248, s[80:81] offset:2048 nt\n"
    "v_xor_b32 v249, 0x30, v250\n"
    "global_load_lds_dwordx4 v249, s[80:81] offset:3072 nt\n"
    "s_add_u32 s80, s80, 0x1000\n"
    "s_addc_u32 s81, s81, 0\n"
    "s_add_u32 m0, m0, 0x1000\n"
    "v_xor_b32 v248, 0x80, v250\n"
    "global_load_lds_dwordx4 v248, s[80:81] offset:0 nt\n"
    "v_xor_b32 v249, 0xa0, v250\n"
    "global_load_lds_dwordx4 v249, s[80:81] offset:1024 nt\n"
    "v_xor_b32 v248, 0x90, v250\n"
    "global_load_lds_dwordx4 v248, s[80:81] offset:2048 nt\n"
    "v_xor_b32 v249, 0xb0, v250\n"
    "global_load_lds_dwordx4 v249, s[80:81] offset:3072 nt\n"
    "v_mov_b32 v254, 0\n"
    "v_mov_b32 v255, 0\n"
    "s_lshl_b32 s56, s59, 7\n"
    "v_mov_b32 v253, s56\n"
    "s_mov_b64 exec, 1\n"
    "global_atomic_add_x2 v[202:203], v253, v[254:255], %[ctl] offset:1024 sc0\n"
    "s_mov_b64 exec, -1\n"
    "s_mov_b64 s[40:41], %[kv]\n"
    "s_mov_b64 s[44:45], %[o]\n"
    "s_and_b32 s41, s41, 0xffff\n"
    "s_mov_b32 s43, 0x20000\n"
    "s_and_b32 s45, s45, 0xffff\n"
    "s_mov_b32 s46, 0x4000\n"
    "s_mov_b32 s47, 0x20000\n"
    "s_mov_b32 s64, 0x3e0293ee\n"
    "s_mov_b32 s65, 0x3e0293ee\n"
    "v_add_u32 v199, 0x1000, v198\n"
    "v_add_u32 v197, s60, v198\n"
    "s_lshl_b32 s56, s59, 13\n"
    "s_add_u32 s51, s56, s60\n"
    "s_mov_b64 s[38:39], %[kv]\n"
    "s_add_u32 s38, s38, s56\n"
    "s_addc_u32 s39, s39, 0\n"
    "s_lshl_b32 s50, s61, 15\n"
    "s_add_u32 s50, s50, s56\n"
    "v_add_u32 v200, s51, v198\n"
    "v_and_b32 v208, 10, v205\n"
    "v_bfe_u32 v209, v205, 2, 1\n"
    "v_or_b32 v208, v208, v209\n"
    "v_and_b32 v209, 1, v205\n"
    "v_lshl_or_b32 v208, v209, 2, v208\n"
    "v_lshlrev_b32 v209, 1, v206\n"
    "v_xor_b32 v208, v208, v209\n"
    "v_lshlrev_b32 v208, 4, v208\n"
    "v_lshl_add_u32 v208, v205, 9, v208\n"
    "v_and_b32 v209, 1, v206\n"
    "v_lshrrev_b32 v210, 1, v206\n"
    "v_lshl_or_b32 v209, v210, 3, v209\n"
    "v_lshlrev_b32 v210, 1, v205\n"
    "v_xor_b32 v209, v209, v210\n"
    "v_lshlrev_b32 v209, 4, v209\n"
    "v_lshl_add_u32 v209, v206, 11, v209\n"
    "s_bitcmp1_b32 s59, 0\n"
    "s_cselect_b64 vcc, -1, 0\n"
    "v_cndmask_b32 v201, v208, v209, vcc\n"
    "v_add_u32 v201, s79, v201\n"
    "v_lshrrev_b32 v144, 8, v198\n"
    "v_lshlrev_b32 v144, 4, v144\n"
    "v_xor_b32 v144, v144, v198\n"
    "v_xor_b32 v145, 0x40, v144\n"
    "v_xor_b32 v146, 0x80, v144\n"
    "v_xor_b32 v147, 0xc0, v144\n"
    "s_mov_b64 s[48:49], %[q]\n"
    "s_add_u32 m0, s51, 32768\n"
    "s_nop 0\n"
    "global_load_lds_dwordx4 v144, s[48:49] offset:0 nt\n"
    "s_add_u32 m0, s51, 32768\n"
    "s_nop 0\n"
    "global_load_lds_dwordx4 v145, s[48:49] offset:1024 nt\n"
    "s_add_u32 m0, s51, 32768\n"
    "s_nop 0\n"
    "global_load_lds_dwordx4 v146, s[48:49] offset:2048 nt\n"
    "s_add_u32 m0, s51, 32768\n"
    "s_nop 0\n"
    "global_load_lds_dwordx4 v147, s[48:49] offset:3072 nt\n"
    "s_add_u32 s48, s48, 0x1000\n"
    "s_addc_u32 s49, s49, 0\n"
    "s_add_u32 m0, s51, 36864\n"
    "s_nop 0\n"
    "global_load_lds_dwordx4 v144, s[48:49] offset:0 nt\n"
    "s_add_u32 m0, s51, 36864\n"
    "s_nop 0\n"
    "global_load_lds_dwordx4 v145, s[48:49] offset:1024 nt\n"
    "s_add_u32 m0, s51, 36864\n"
    "s_nop 0\n"
    "global_load_lds_dwordx4 v146, s[48:49] offset:2048 nt\n"
    "s_add_u32 m0, s51, 36864\n"
    "s_nop 0\n"
    "global_load_lds_dwordx4 v147, s[48:49] offset:3072 nt\n"
    "s_waitcnt vmcnt(25)\n"
    "v_lshlrev_b32 v0, 16, v64\n"
    "v_and_b32 v1, 0xffff0000, v64\n"
    "v_lshlrev_b32 v8, 16, v68\n"
    "v_and_b32 v9, 0xffff0000, v68\n"
    "v_lshlrev_b32 v2, 16, v65\n"
    "v_and_b32 v3, 0xffff0000, v65\n"
    "v_lshlrev_b32 v10, 16, v69\n"
    "v_and_b32 v11, 0xffff0000, v69\n"
    "v_lshlrev_b32 v4, 16, v66\n"
    "v_and_b32 v5, 0xffff0000, v66\n"
    "v_lshlrev_b32 v12, 16, v70\n"
    "v_and_b32 v13, 0xffff0000, v70\n"
    "v_lshlrev_b32 v6, 16, v67\n"
    "v_and_b32 v7, 0xffff0000, v67\n"
    "v_lshlrev_b32 v14, 16, v71\n"
    "v_and_b32 v15, 0xffff0000, v71\n"
    "v_xor_b32 v236, 0x0, v201\n"
    "v_xor_b32 v237, 0x10, v201\n"
    "v_xor_b32 v238, 0x40, v201\n"
    "v_xor_b32 v239, 0x50, v201\n"
    "v_xor_b32 v240, 0x20, v201\n"
    "v_xor_b32 v241, 0x30, v201\n"
    "v_xor_b32 v242, 0x60, v201\n"
    "v_xor_b32 v243, 0x70, v201\n"
    "s_waitcnt vmcnt(17)\n"
    "ds_read_b128 v[220:223], v236 offset:0\n"
    "ds_read_b128 v[224:227], v238 offset:512\n"
    "ds_read_b128 v[228:231], v240 offset:1024\n"
    "ds_read_b128 v[232:235], v242 offset:1536\n"
    "s_waitcnt lgkmcnt(2)\n"
    "v_cvt_f32_i32 v220, v220\n"
    "v_cvt_f32_i32 v221, v221\n"
    "v_cvt_f32_i32 v222, v222\n"
    "v_cvt_f32_i32 v223, v223\n"
    "v_cvt_f32_i32 v224, v224\n"
    "v_cvt_f32_i32 v225, v225\n"
    "v_cvt_f32_i32 v226, v226\n"
    "v_cvt_f32_i32 v227, v227\n"
    "v_cvt_pk_bf16_f32 v220, 0, v220\n"
    "v_cvt_pk_bf16_f32 v221, 0, v221\n"
    "v_cvt_pk_bf16_f32 v222, 0, v222\n"
    "v_cvt_pk_bf16_f32 v223, 0, v223\n"
    "v_cvt_pk_bf16_f32 v224, 0, v224\n"
    "v_cvt_pk_bf16_f32 v225, 0, v225\n"
    "v_cvt_pk_bf16_f32 v226, 0, v226\n"
    "v_cvt_pk_bf16_f32 v227, 0, v227\n"
    "v_pk_add_f32 v[220:221], v[220:221], v[8:9] op_sel_hi:[1,0] neg_lo:[0,1] neg_hi:[0,1]\n"
    "v_pk_add_f32 v[222:223], v[222:223], v[8:9] op_sel_hi:[1,0] neg_lo:[0,1] neg_hi:[0,1]\n"
    "v_pk_add_f32 v[224:225], v[224:225], v[8:9] op_sel:[0,1] neg_lo:[0,1] neg_hi:[0,1]\n"
    "v_pk_add_f32 v[226:227], v[226:227], v[8:9] op_sel:[0,1] neg_lo:[0,1] neg_hi:[0,1]\n"
    "v_cvt_pk_bf16_f32 v220, 0, v220\n"
    "v_cvt_pk_bf16_f32 v221, 0, v221\n"
    "v_cvt_pk_bf16_f32 v222, 0, v222\n"
    "v_cvt_pk_bf16_f32 v223, 0, v223\n"
    "v_cvt_pk_bf16_f32 v224, 0, v224\n"
    "v_cvt_pk_bf16_f32 v225, 0, v225\n"
    "v_cvt_pk_bf16_f32 v226, 0, v226\n"
    "v_cvt_pk_bf16_f32 v227, 0, v227\n"
    "v_pk_mul_f32 v[220:221], v[220:221], v[0:1] op_sel_hi:[1,0]\n"
    "v_pk_mul_f32 v[222:223], v[222:223], v[0:1] op_sel_hi:[1,0]\n"
    "v_pk_mul_f32 v[224:225], v[224:225], v[0:1] op_sel:[0,1]\n"
    "v_pk_mul_f32 v[226:227], v[226:227], v[0:1] op_sel:[0,1]\n"
    "v_cvt_pk_bf16_f32 v16, v220, v224\n"
    "v_cvt_pk_bf16_f32 v20, v221, v225\n"
    "v_cvt_pk_bf16_f32 v24, v222, v226\n"
    "v_cvt_pk_bf16_f32 v28, v223, v227\n"
    "s_cmp_lt_u32 s61, 6\n"
    "s_cbranch_scc1 .Lhe8\n"
    "s_add_u32 s48, s48, 0x1000\n"
    "s_addc_u32 s49, s49, 0\n"
    "s_add_u32 m0, s51, 65536\n"
    "s_nop 0\n"
    "global_load_lds_dwordx4 v144, s[48:49] offset:0 nt\n"
    "s_add_u32 m0, s51, 65536\n"
    "s_nop 0\n"
    "global_load_lds_dwordx4 v145, s[48:49] offset:1024 nt\n"
    "s_branch .Lhj8\n"
    ".Lhe8:\n"
    ".Lhj8:\n"
    "ds_read_b128 v[220:223], v237 offset:0\n"
    "ds_read_b128 v[224:227], v239 offset:512\n"
    "s_waitcnt lgkmcnt(2)\n"
    "v_cvt_f32_i32 v228, v228\n"
    "v_cvt_f32_i32 v229, v229\n"
    "v_cvt_f32_i32 v230, v230\n"
    "v_cvt_f32_i32 v231, v231\n"
    "v_cvt_f32_i32 v232, v232\n"
    "v_cvt_f32_i32 v233, v233\n"
    "v_cvt_f32_i32 v234, v234\n"
    "v_cvt_f32_i32 v235, v235\n"
    "v_cvt_pk_bf16_f32 v228, 0, v228\n"
    "v_cvt_pk_bf16_f32 v229, 0, v229\n"
    "v_cvt_pk_bf16_f32 v230, 0, v230\n"
    "v_cvt_pk_bf16_f32 v231, 0, v231\n"
    "v_cvt_pk_bf16_f32 v232, 0, v232\n"
    "v_cvt_pk_bf16_f32 v233, 0, v233\n"
    "v_cvt_pk_bf16_f32 v234, 0, v234\n"
    "v_cvt_pk_bf16_f32 v235, 0, v235\n"
    "v_pk_add_f32 v[228:229], v[228:229], v[10:11] op_sel_hi:[1,0] neg_lo:[0,1] neg_hi:[0,1]\n"
    "v_pk_add_f32 v[230:231], v[230:231], v[10:11] op_sel_hi:[1,0] neg_lo:[0,1] neg_hi:[0,1]\n"
    "v_pk_add_f32 v[232:233], v[232:233], v[10:11] op_sel:[0,1] neg_lo:[0,1] neg_hi:[0,1]\n"
    "v_pk_add_f32 v[234:235], v[234:235], v[10:11] op_sel:[0,1] neg_lo:[0,1] neg_hi:[0,1]\n"
    "v_cvt_pk_bf16_f32 v228, 0, v228\n"
    "v_cvt_pk_bf16_f32 v229, 0, v229\n"
    "v_cvt_pk_bf16_f32 v230, 0, v230\n"
    "v_cvt_pk_bf16_f32 v231, 0, v231\n"
    "v_cvt_pk_bf16_f32 v232, 0, v232\n"
    "v_cvt_pk_bf16_f32 v233, 0, v233\n"
    "v_cvt_pk_bf16_f32 v234, 0, v234\n"
    "v_cvt_pk_bf16_f32 v235, 0, v235\n"
    "v_pk_mul_f32 v[228:229], v[228:229], v[2:3] op_sel_hi:[1,0]\n"
    "v_pk_mul_f32 v[230:231], v[230:231], v[2:3] op_sel_hi:[1,0]\n"
    "v_pk_mul_f32 v[232:233], v[232:233], v[2:3] op_sel:[0,1]\n"
    "v_pk_mul_f32 v[234:235], v[234:235], v[2:3] op_sel:[0,1]\n"
    "v_cvt_pk_bf16_f32 v17, v228, v232\n"
    "v_cvt_pk_bf16_f32 v21, v229, v233\n"
    "v_cvt_pk_bf16_f32 v25, v230, v234\n"
    "v_cvt_pk_bf16_f32 v29, v231, v235\n"
    "s_cmp_lt_u32 s61, 6\n"
    "s_cbranch_scc1 .Lhe9\n"
    "s_add_u32 m0, s51, 65536\n"
    "s_nop 0\n"
    "global_load_lds_dwordx4 v146, s[48:49] offset:2048 nt\n"
    "s_add_u32 m0, s51, 65536\n"
    "s_nop 0\n"
    "global_load_lds_dwordx4 v147, s[48:49] offset:3072 nt\n"
    "s_add_u32 s48, s48, 0x1000\n"
    "s_addc_u32 s49, s49, 0\n"
    "s_add_u32 m0, s51, 69632\n"
    "s_nop 0\n"
    "global_load_lds_dwordx4 v144, s[48:49] offset:0 nt\n"
    "s_branch .Lhj9\n"
    ".Lhe9:\n"
    ".Lhj9:\n"
    "ds_read_b128 v[228:231], v241 offset:1024\n"
    "ds_read_b128 v[232:235], v243 offset:1536\n"
    "s_waitcnt lgkmcnt(2)\n"
    "v_cvt_f32_i32 v220, v220\n"
    "v_cvt_f32_i32 v221, v221\n"
    "v_cvt_f32_i32 v222, v222\n"
    "v_cvt_f32_i32 v223, v223\n"
    "v_cvt_f32_i32 v224, v224\n"
    "v_cvt_f32_i32 v225, v225\n"
    "v_cvt_f32_i32 v226, v226\n"
    "v_cvt_f32_i32 v227, v227\n"
    "v_cvt_pk_bf16_f32 v220, 0, v220\n"
    "v_cvt_pk_bf16_f32 v221, 0, v221\n"
    "v_cvt_pk_bf16_f32 v222, 0, v222\n"
    "v_cvt_pk_bf16_f32 v223, 0, v223\n"
    "v_cvt_pk_bf16_f32 v224, 0, v224\n"
    "v_cvt_pk_bf16_f32 v225, 0, v225\n"
    "v_cvt_pk_bf16_f32 v226, 0, v226\n"
    "v_cvt_pk_bf16_f32 v227, 0, v227\n"
    "v_pk_add_f32 v[220:221], v[220:221], v[8:9] op_sel_hi:[1,0] neg_lo:[0,1] neg_hi:[0,1]\n"
    "v_pk_add_f32 v[222:223], v[222:223], v[8:9] op_sel_hi:[1,0] neg_lo:[0,1] neg_hi:[0,1]\n"
    "v_pk_add_f32 v[224:225], v[224:225], v[8:9] op_sel:[0,1] neg_lo:[0,1] neg_hi:[0,1]\n"
    "v_pk_add_f32 v[226:227], v[226:227], v[8:9] op_sel:[0,1] neg_lo:[0,1] neg_hi:[0,1]\n"
    "v_cvt_pk_bf16_f32 v220, 0, v220\n"
    "v_cvt_pk_bf16_f32 v221, 0, v221\n"
    "v_cvt_pk_bf16_f32 v222, 0, v222\n"
    "v_cvt_pk_bf16_f32 v223, 0, v223\n"
    "v_cvt_pk_bf16_f32 v224, 0, v224\n"
    "v_cvt_pk_bf16_f32 v225, 0, v225\n"
    "v_cvt_pk_bf16_f32 v226, 0, v226\n"
    "v_cvt_pk_bf16_f32 v227, 0, v227\n"
    "v_pk_mul_f32 v[220:221], v[220:221], v[0:1] op_sel_hi:[1,0]\n"
    "v_pk_mul_f32 v[222:223], v[222:223], v[0:1] op_sel_hi:[1,0]\n"
    "v_pk_mul_f32 v[224:225], v[224:225], v[0:1] op_sel:[0,1]\n"
    "v_pk_mul_f32 v[226:227], v[226:227], v[0:1] op_sel:[0,1]\n"
    "v_cvt_pk_bf16_f32 v32, v220, v224\n"
    "v_cvt_pk_bf16_f32 v36, v221, v225\n"
    "v_cvt_pk_bf16_f32 v40, v222, v226\n"
    "v_cvt_pk_bf16_f32 v44, v223, v227\n"
    "s_cmp_lt_u32 s61, 6\n"
    "s_cbranch_scc1 .Lhe10\n"
    "s_add_u32 m0, s51, 69632\n"
    "s_nop 0\n"
    "global_load_lds_dwordx4 v145, s[48:49] offset:1024 nt\n"
    "s_add_u32 m0, s51, 69632\n"
    "s_nop 0\n"
    "global_load_lds_dwordx4 v146, s[48:49] offset:2048 nt\n"
    "s_add_u32 m0, s51, 69632\n"
    "s_nop 0\n"
    "global_load_lds_dwordx4 v147, s[48:49] offset:3072 nt\n"
    "s_waitcnt vmcnt(17)\n"
    "s_branch .Lhj10\n"
    ".Lhe10:\n"
    "s_waitcnt vmcnt(9)\n"
    ".Lhj10:\n"
    "ds_read_b128 v[220:223], v236 offset:8192\n"
    "ds_read_b128 v[224:227], v238 offset:8704\n"
    "s_waitcnt lgkmcnt(2)\n"
    "v_cvt_f32_i32 v228, v228\n"
    "v_cvt_f32_i32 v229, v229\n"
    "v_cvt_f32_i32 v230, v230\n"
    "v_cvt_f32_i32 v231, v231\n"
    "v_cvt_f32_i32 v232, v232\n"
    "v_cvt_f32_i32 v233, v233\n"
    "v_cvt_f32_i32 v234, v234\n"
    "v_cvt_f32_i32 v235, v235\n"
    "v_cvt_pk_bf16_f32 v228, 0, v228\n"
    "v_cvt_pk_bf16_f32 v229, 0, v229\n"
    "v_cvt_pk_bf16_f32 v230, 0, v230\n"
    "v_cvt_pk_bf16_f32 v231, 0, v231\n"
    "v_cvt_pk_bf16_f32 v232, 0, v232\n"
    "v_cvt_pk_bf16_f32 v233, 0, v233\n"
    "v_cvt_pk_bf16_f32 v234, 0, v234\n"
    "v_cvt_pk_bf16_f32 v235, 0, v235\n"
    "v_pk_add_f32 v[228:229], v[228:229], v[10:11] op_sel_hi:[1,0] neg_lo:[0,1] neg_hi:[0,1]\n"
    "v_pk_add_f32 v[230:231], v[230:231], v[10:11] op_sel_hi:[1,0] neg_lo:[0,1] neg_hi:[0,1]\n"
    "v_pk_add_f32 v[232:233], v[232:233], v[10:11] op_sel:[0,1] neg_lo:[0,1] neg_hi:[0,1]\n"
    "v_pk_add_f32 v[234:235], v[234:235], v[10:11] op_sel:[0,1] neg_lo:[0,1] neg_hi:[0,1]\n"
    "v_cvt_pk_bf16_f32 v228, 0, v228\n"
    "v_cvt_pk_bf16_f32 v229, 0, v229\n"
    "v_cvt_pk_bf16_f32 v230, 0, v230\n"
    "v_cvt_pk_bf16_f32 v231, 0, v231\n"
    "v_cvt_pk_bf16_f32 v232, 0, v232\n"
    "v_cvt_pk_bf16_f32 v233, 0, v233\n"
    "v_cvt_pk_bf16_f32 v234, 0, v234\n"
    "v_cvt_pk_bf16_f32 v235, 0, v235\n"
    "v_pk_mul_f32 v[228:229], v[228:229], v[2:3] op_sel_hi:[1,0]\n"
    "v_pk_mul_f32 v[230:231], v[230:231], v[2:3] op_sel_hi:[1,0]\n"
    "v_pk_mul_f32 v[232:233], v[232:233], v[2:3] op_sel:[0,1]\n"
    "v_pk_mul_f32 v[234:235], v[234:235], v[2:3] op_sel:[0,1]\n"
    "v_cvt_pk_bf16_f32 v33, v228, v232\n"
    "v_cvt_pk_bf16_f32 v37, v229, v233\n"
    "v_cvt_pk_bf16_f32 v41, v230, v234\n"
    "v_cvt_pk_bf16_f32 v45, v231, v235\n"
    "ds_read_b128 v[228:231], v240 offset:9216\n"
    "ds_read_b128 v[232:235], v242 offset:9728\n"
    "s_waitcnt lgkmcnt(2)\n"
    "v_cvt_f32_i32 v220, v220\n"
    "v_cvt_f32_i32 v221, v221\n"
    "v_cvt_f32_i32 v222, v222\n"
    "v_cvt_f32_i32 v223, v223\n"
    "v_cvt_f32_i32 v224, v224\n"
    "v_cvt_f32_i32 v225, v225\n"
    "v_cvt_f32_i32 v226, v226\n"
    "v_cvt_f32_i32 v227, v227\n"
    "v_cvt_pk_bf16_f32 v220, 0, v220\n"
    "v_cvt_pk_bf16_f32 v221, 0, v221\n"
    "v_cvt_pk_bf16_f32 v222, 0, v222\n"
    "v_cvt_pk_bf16_f32 v223, 0, v223\n"
    "v_cvt_pk_bf16_f32 v224, 0, v224\n"
    "v_cvt_pk_bf16_f32 v225, 0, v225\n"
    "v_cvt_pk_bf16_f32 v226, 0, v226\n"
    "v_cvt_pk_bf16_f32 v227, 0, v227\n"
    "v_pk_add_f32 v[220:221], v[220:221], v[12:13] op_sel_hi:[1,0] neg_lo:[0,1] neg_hi:[0,1]\n"
    "v_pk_add_f32 v[222:223], v[222:223], v[12:13] op_sel_hi:[1,0] neg_lo:[0,1] neg_hi:[0,1]\n"
    "v_pk_add_f32 v[224:225], v[224:225], v[12:13] op_sel:[0,1] neg_lo:[0,1] neg_hi:[0,1]\n"
    "v_pk_add_f32 v[226:227], v[226:227], v[12:13] op_sel:[0,1] neg_lo:[0,1] neg_hi:[0,1]\n"
    "v_cvt_pk_bf16_f32 v220, 0, v220\n"
    "v_cvt_pk_bf16_f32 v221, 0, v221\n"
    "v_cvt_pk_bf16_f32 v222, 0, v222\n"
    "v_cvt_pk_bf16_f32 v223, 0, v223\n"
    "v_cvt_pk_bf16_f32 v224, 0, v224\n"
    "v_cvt_pk_bf16_f32 v225, 0, v225\n"
    "v_cvt_pk_bf16_f32 v226, 0, v226\n"
    "v_cvt_pk_bf16_f32 v227, 0, v227\n"
    "v_pk_mul_f32 v[220:221], v[220:221], v[4:5] op_sel_hi:[1,0]\n"
    "v_pk_mul_f32 v[222:223], v[222:223], v[4:5] op_sel_hi:[1,0]\n"
    "v_pk_mul_f32 v[224:225], v[224:225], v[4:5] op_sel:[0,1]\n"
    "v_pk_mul_f32 v[226:227], v[226:227], v[4:5] op_sel:[0,1]\n"
    "v_cvt_pk_bf16_f32 v18, v220, v224\n"
    "v_cvt_pk_bf16_f32 v22, v221, v225\n"
    "v_cvt_pk_bf16_f32 v26, v222, v226\n"
    "v_cvt_pk_bf16_f32 v30, v223, v227\n"
    "ds_read_b128 v[220:223], v237 offset:8192\n"
    "ds_read_b128 v[224:227], v239 offset:8704\n"
    "s_waitcnt lgkmcnt(2)\n"
    "v_cvt_f32_i32 v228, v228\n"
    "v_cvt_f32_i32 v229, v229\n"
    "v_cvt_f32_i32 v230, v230\n"
    "v_cvt_f32_i32 v231, v231\n"
    "v_cvt_f32_i32 v232, v232\n"
    "v_cvt_f32_i32 v233, v233\n"
    "v_cvt_f32_i32 v234, v234\n"
    "v_cvt_f32_i32 v235, v235\n"
    "v_cvt_pk_bf16_f32 v228, 0, v228\n"
    "v_cvt_pk_bf16_f32 v229, 0, v229\n"
    "v_cvt_pk_bf16_f32 v230, 0, v230\n"
    "v_cvt_pk_bf16_f32 v231, 0, v231\n"
    "v_cvt_pk_bf16_f32 v232, 0, v232\n"
    "v_cvt_pk_bf16_f32 v233, 0, v233\n"
    "v_cvt_pk_bf16_f32 v234, 0, v234\n"
    "v_cvt_pk_bf16_f32 v235, 0, v235\n"
    "v_pk_add_f32 v[228:229], v[228:229], v[14:15] op_sel_hi:[1,0] neg_lo:[0,1] neg_hi:[0,1]\n"
    "v_pk_add_f32 v[230:231], v[230:231], v[14:15] op_sel_hi:[1,0] neg_lo:[0,1] neg_hi:[0,1]\n"
    "v_pk_add_f32 v[232:233], v[232:233], v[14:15] op_sel:[0,1] neg_lo:[0,1] neg_hi:[0,1]\n"
    "v_pk_add_f32 v[234:235], v[234:235], v[14:15] op_sel:[0,1] neg_lo:[0,1] neg_hi:[0,1]\n"
    "v_cvt_pk_bf16_f32 v228, 0, v228\n"
    "v_cvt_pk_bf16_f32 v229, 0, v229\n"
    "v_cvt_pk_bf16_f32 v230, 0, v230\n"
    "v_cvt_pk_bf16_f32 v231, 0, v231\n"
    "v_cvt_pk_bf16_f32 v232, 0, v232\n"
    "v_cvt_pk_bf16_f32 v233, 0, v233\n"
    "v_cvt_pk_bf16_f32 v234, 0, v234\n"
    "v_cvt_pk_bf16_f32 v235, 0, v235\n"
    "v_pk_mul_f32 v[228:229], v[228:229], v[6:7] op_sel_hi:[1,0]\n"
    "v_pk_mul_f32 v[230:231], v[230:231], v[6:7] op_sel_hi:[1,0]\n"
    "v_pk_mul_f32 v[232:233], v[232:233], v[6:7] op_sel:[0,1]\n"
    "v_pk_mul_f32 v[234:235], v[234:235], v[6:7] op_sel:[0,1]\n"
    "v_cvt_pk_bf16_f32 v19, v228, v232\n"
    "v_cvt_pk_bf16_f32 v23, v229, v233\n"
    "v_cvt_pk_bf16_f32 v27, v230, v234\n"
    "v_cvt_pk_bf16_f32 v31, v231, v235\n"
    "ds_write_b128 v200, v[16:19] offset:0\n"
    "ds_write_b128 v200, v[20:23] offset:1024\n"
    "ds_write_b128 v200, v[24:27] offset:2048\n"
    "ds_write_b128 v200, v[28:31] offset:3072\n"
    "s_cmp_lt_u32 s61, 6\n"
    "s_cbranch_scc1 .Lhe11\n"
    "v_accvgpr_write_b32 a192, v16\n"
    "v_accvgpr_write_b32 a193, v17\n"
    "v_accvgpr_write_b32 a194, v18\n"
    "v_accvgpr_write_b32 a195, v19\n"
    "v_accvgpr_write_b32 a196, v20\n"
    "v_accvgpr_write_b32 a197, v21\n"
    "v_accvgpr_write_b32 a198, v22\n"
    "v_accvgpr_write_b32 a199, v23\n"
    "v_accvgpr_write_b32 a200, v24\n"
    "v_accvgpr_write_b32 a201, v25\n"
    "v_accvgpr_write_b32 a202, v26\n"
    "v_accvgpr_write_b32 a203, v27\n"
    "v_accvgpr_write_b32 a204, v28\n"
    "v_accvgpr_write_b32 a205, v29\n"
    "v_accvgpr_write_b32 a206, v30\n"
    "v_accvgpr_write_b32 a207, v31\n"
    "s_branch .Lhj11\n"
    ".Lhe11:\n"
    "buffer_store_dwordx4 v[16:19], v198, s[40:43], s50 offen offset:0 sc1\n"
    "buffer_store_dwordx4 v[20:23], v198, s[40:43], s50 offen offset:1024 sc1\n"
    "buffer_store_dwordx4 v[24:27], v198, s[40:43], s50 offen offset:2048 sc1\n"
    "buffer_store_dwordx4 v[28:31], v198, s[40:43], s50 offen offset:3072 sc1\n"
    ".Lhj11:\n"
    "ds_read_b128 v[228:231], v241 offset:9216\n"
    "ds_read_b128 v[232:235], v243 offset:9728\n"
    "s_waitcnt lgkmcnt(2)\n"
    "v_cvt_f32_i32 v220, v220\n"
    "v_cvt_f32_i32 v221, v221\n"
    "v_cvt_f32_i32 v222, v222\n"
    "v_cvt_f32_i32 v223, v223\n"
    "v_cvt_f32_i32 v224, v224\n"
    "v_cvt_f32_i32 v225, v225\n"
    "v_cvt_f32_i32 v226, v226\n"
    "v_cvt_f32_i32 v227, v227\n"
    "v_cvt_pk_bf16_f32 v220, 0, v220\n"
    "v_cvt_pk_bf16_f32 v221, 0, v221\n"
    "v_cvt_pk_bf16_f32 v222, 0, v222\n"
    "v_cvt_pk_bf16_f32 v223, 0, v223\n"
    "v_cvt_pk_bf16_f32 v224, 0, v224\n"
    "v_cvt_pk_bf16_f32 v225, 0, v225\n"
    "v_cvt_pk_bf16_f32 v226, 0, v226\n"
    "v_cvt_pk_bf16_f32 v227, 0, v227\n"
    "v_pk_add_f32 v[220:221], v[220:221], v[12:13] op_sel_hi:[1,0] neg_lo:[0,1] neg_hi:[0,1]\n"
    "v_pk_add_f32 v[222:223], v[222:223], v[12:13] op_sel_hi:[1,0] neg_lo:[0,1] neg_hi:[0,1]\n"
    "v_pk_add_f32 v[224:225], v[224:225], v[12:13] op_sel:[0,1] neg_lo:[0,1] neg_hi:[0,1]\n"
    "v_pk_add_f32 v[226:227], v[226:227], v[12:13] op_sel:[0,1] neg_lo:[0,1] neg_hi:[0,1]\n"
    "v_cvt_pk_bf16_f32 v220, 0, v220\n"
    "v_cvt_pk_bf16_f32 v221, 0, v221\n"
    "v_cvt_pk_bf16_f32 v222, 0, v222\n"
    "v_cvt_pk_bf16_f32 v223, 0, v223\n"
    "v_cvt_pk_bf16_f32 v224, 0, v224\n"
    "v_cvt_pk_bf16_f32 v225, 0, v225\n"
    "v_cvt_pk_bf16_f32 v226, 0, v226\n"
    "v_cvt_pk_bf16_f32 v227, 0, v227\n"
    "v_pk_mul_f32 v[220:221], v[220:221], v[4:5] op_sel_hi:[1,0]\n"
    "v_pk_mul_f32 v[222:223], v[222:223], v[4:5] op_sel_hi:[1,0]\n"
    "v_pk_mul_f32 v[224:225], v[224:225], v[4:5] op_sel:[0,1]\n"
    "v_pk_mul_f32 v[226:227], v[226:227], v[4:5] op_sel:[0,1]\n"
    "v_cvt_pk_bf16_f32 v34, v220, v224\n"
    "v_cvt_pk_bf16_f32 v38, v221, v225\n"
    "v_cvt_pk_bf16_f32 v42, v222, v226\n"
    "v_cvt_pk_bf16_f32 v46, v223, v227\n"
    "s_waitcnt lgkmcnt(0)\n"
    "v_cvt_f32_i32 v228, v228\n"
    "v_cvt_f32_i32 v229, v229\n"
    "v_cvt_f32_i32 v230, v230\n"
    "v_cvt_f32_i32 v231, v231\n"
    "v_cvt_f32_i32 v232, v232\n"
    "v_cvt_f32_i32 v233, v233\n"
    "v_cvt_f32_i32 v234, v234\n"
    "v_cvt_f32_i32 v235, v235\n"
    "v_cvt_pk_bf16_f32 v228, 0, v228\n"
    "v_cvt_pk_bf16_f32 v229, 0, v229\n"
    "v_cvt_pk_bf16_f32 v230, 0, v230\n"
    "v_cvt_pk_bf16_f32 v231, 0, v231\n"
    "v_cvt_pk_bf16_f32 v232, 0, v232\n"
    "v_cvt_pk_bf16_f32 v233, 0, v233\n"
    "v_cvt_pk_bf16_f32 v234, 0, v234\n"
    "v_cvt_pk_bf16_f32 v235, 0, v235\n"
    "v_pk_add_f32 v[228:229], v[228:229], v[14:15] op_sel_hi:[1,0] neg_lo:[0,1] neg_hi:[0,1]\n"
    "v_pk_add_f32 v[230:231], v[230:231], v[14:15] op_sel_hi:[1,0] neg_lo:[0,1] neg_hi:[0,1]\n"
    "v_pk_add_f32 v[232:233], v[232:233], v[14:15] op_sel:[0,1] neg_lo:[0,1] neg_hi:[0,1]\n"
    "v_pk_add_f32 v[234:235], v[234:235], v[14:15] op_sel:[0,1] neg_lo:[0,1] neg_hi:[0,1]\n"
    "v_cvt_pk_bf16_f32 v228, 0, v228\n"
    "v_cvt_pk_bf16_f32 v229, 0, v229\n"
    "v_cvt_pk_bf16_f32 v230, 0, v230\n"
    "v_cvt_pk_bf16_f32 v231, 0, v231\n"
    "v_cvt_pk_bf16_f32 v232, 0, v232\n"
    "v_cvt_pk_bf16_f32 v233, 0, v233\n"
    "v_cvt_pk_bf16_f32 v234, 0, v234\n"
    "v_cvt_pk_bf16_f32 v235, 0, v235\n"
    "v_pk_mul_f32 v[228:229], v[228:229], v[6:7] op_sel_hi:[1,0]\n"
    "v_pk_mul_f32 v[230:231], v[230:231], v[6:7] op_sel_hi:[1,0]\n"
    "v_pk_mul_f32 v[232:233], v[232:233], v[6:7] op_sel:[0,1]\n"
    "v_pk_mul_f32 v[234:235], v[234:235], v[6:7] op_sel:[0,1]\n"
    "v_cvt_pk_bf16_f32 v35, v228, v232\n"
    "v_cvt_pk_bf16_f32 v39, v229, v233\n"
    "v_cvt_pk_bf16_f32 v43, v230, v234\n"
    "v_cvt_pk_bf16_f32 v47, v231, v235\n"
    "ds_write_b128 v200, v[32:35] offset:4096\n"
    "ds_write_b128 v200, v[36:39] offset:5120\n"
    "ds_write_b128 v200, v[40:43] offset:6144\n"
    "ds_write_b128 v200, v[44:47] offset:7168\n"
    "s_cmp_lt_u32 s61, 6\n"
    "s_cbranch_scc1 .Lhe12\n"
    "v_accvgpr_write_b32 a208, v32\n"
    "v_accvgpr_write_b32 a209, v33\n"
    "v_accvgpr_write_b32 a210, v34\n"
    "v_accvgpr_write_b32 a211, v35\n"
    "v_accvgpr_write_b32 a212, v36\n"
    "v_accvgpr_write_b32 a213, v37\n"
    "v_accvgpr_write_b32 a214, v38\n"
    "v_accvgpr_write_b32 a215, v39\n"
    "v_accvgpr_write_b32 a216, v40\n"
    "v_accvgpr_write_b32 a217, v41\n"
    "v_accvgpr_write_b32 a218, v42\n"
    "v_accvgpr_write_b32 a219, v43\n"
    "v_accvgpr_write_b32 a220, v44\n"
    "v_accvgpr_write_b32 a221, v45\n"
    "v_accvgpr_write_b32 a222, v46\n"
    "v_accvgpr_write_b32 a223, v47\n"
    "s_branch .Lhj12\n"
    ".Lhe12:\n"
    "buffer_store_dwordx4 v[32:35], v199, s[40:43], s50 offen offset:0 sc1\n"
    "buffer_store_dwordx4 v[36:39], v199, s[40:43], s50 offen offset:1024 sc1\n"
    "buffer_store_dwordx4 v[40:43], v199, s[40:43], s50 offen offset:2048 sc1\n"
    "buffer_store_dwordx4 v[44:47], v199, s[40:43], s50 offen offset:3072 sc1\n"
    ".Lhj12:\n"
    ".Lrole_done:\n"
    "s_mov_b64 s[88:89], s[40:41]\n"
    "s_mov_b32 s90, 1048576\n"
    "s_mov_b32 s91, 0x20000\n"
    "s_cmp_lt_u32 s61, 6\n"
    "s_cbranch_scc0 .Lcls_j1\n"
    "s_mov_b32 s90, 0\n"
    "s_waitcnt vmcnt(0)\n"
    "v_readfirstlane_b32 s62, v202\n"
    "v_readfirstlane_b32 s63, v203\n"
    "s_nop 3\n"
    "s_xor_b32 s62, s62, 0x7f4a7c15\n"
    "s_xor_b32 s63, s63, 0x9e3779b9\n"
    "s_lshl_b32 s56, s59, 8\n"
    "s_lshl_b32 s57, s61, 3\n"
    "s_add_u32 s56, s56, s57\n"
    "v_mov_b32 v252, s56\n"
    "v_mov_b32 v254, s62\n"
    "v_mov_b32 v255, s63\n"
    "s_mov_b64 exec, 1\n"
    "global_store_dwordx2 v252, v[254:255], %[ctl] sc1\n"
    "s_mov_b64 exec, -1\n"
    "s_add_u32 s48, s48, 0x1000\n"
    "s_addc_u32 s49, s49, 0\n"
    "s_add_u32 m0, s51, 65536\n"
    "s_nop 0\n"
    "global_load_lds_dwordx4 v144, s[48:49] offset:0 nt\n"
    "s_add_u32 m0, s51, 65536\n"
    "s_nop 0\n"
    "global_load_lds_dwordx4 v145, s[48:49] offset:1024 nt\n"
    "s_add_u32 m0, s51, 65536\n"
    "s_nop 0\n"
    "global_load_lds_dwordx4 v146, s[48:49] offset:2048 nt\n"
    "s_add_u32 m0, s51, 65536\n"
    "s_nop 0\n"
    "global_load_lds_dwordx4 v147, s[48:49] offset:3072 nt\n"
    "s_add_u32 s48, s48, 0x1000\n"
    "s_addc_u32 s49, s49, 0\n"
    "s_add_u32 m0, s51, 69632\n"
    "s_nop 0\n"
    "global_load_lds_dwordx4 v144, s[48:49] offset:0 nt\n"
    "s_add_u32 m0, s51, 69632\n"
    "s_nop 0\n"
    "global_load_lds_dwordx4 v145, s[48:49] offset:1024 nt\n"
    "s_add_u32 m0, s51, 69632\n"
    "s_nop 0\n"
    "global_load_lds_dwordx4 v146, s[48:49] offset:2048 nt\n"
    "s_add_u32 m0, s51, 69632\n"
    "s_nop 0\n"
    "global_load_lds_dwordx4 v147, s[48:49] offset:3072 nt\n"
    ".Lcls_j1:\n"
    "s_waitcnt lgkmcnt(0)\n"
    "s_barrier\n"
    "ds_read_b128 a[64:67], v197 offset:0\n"
    "ds_read_b128 a[68:71], v197 offset:1024\n"
    "ds_read_b128 a[72:75], v197 offset:2048\n"
    "ds_read_b128 a[76:79], v197 offset:3072\n"
    "ds_read_b128 a[80:83], v197 offset:4096\n"
    "ds_read_b128 a[84:87], v197 offset:5120\n"
    "ds_read_b128 a[88:91], v197 offset:6144\n"
    "ds_read_b128 a[92:95], v197 offset:7168\n"
    "ds_read_b128 a[96:99], v197 offset:16384\n"
    "ds_read_b128 a[100:103], v197 offset:17408\n"
    "ds_read_b128 a[104:107], v197 offset:18432\n"
    "ds_read_b128 a[108:111], v197 offset:19456\n"
    "s_add_u32 s56, s60, 0x20000\n"
    "v_mov_b32 v200, s56\n"
    "v_mov_b32 v201, 0\n"
    "ds_write_b32 v200, v201 offset:0\n"
    "s_nop 0\n"
    "v_mov_b32 v201, 1\n"
    "s_mov_b32 s52, 0x18000\n"
    "s_mov_b32 s87, 0\n"
    "s_mov_b32 s53, 0x8000\n"
    "s_mov_b32 s69, -1\n"
    "s_mov_b32 s70, 0\n"
    "s_mov_b32 s55, 0x67800000\n"
    "v_add_u32 v196, s53, v197\n"
    "v_mov_b32 v192, 0\n"
    "v_mov_b32 v193, 0\n"
    "v_mov_b32 v194, 0\n"
    "v_mov_b32 v195, 0\n"
    "v_and_b32 v208, 0xf0, v198\n"
    "v_lshrrev_b32 v209, 8, v198\n"
    "v_lshlrev_b32 v209, 4, v209\n"
    "v_xor_b32 v209, v209, v208\n"
    "v_lshl_add_u32 v209, v208, 4, v209\n"
    "s_add_u32 s56, s51, 0x8000\n"
    "v_add_u32 v210, s56, v209\n"
    "v_xor_b32 v211, 0x40, v210\n"
    "v_xor_b32 v212, 0x80, v210\n"
    "v_xor_b32 v213, 0xc0, v210\n"
    "s_waitcnt lgkmcnt(4)\n"
    "ds_read_b128 a[112:115], v197 offset:20480\n"
    "ds_read_b128 a[116:119], v197 offset:21504\n"
    "ds_read_b128 a[120:123], v197 offset:22528\n"
    "ds_read_b128 a[124:127], v197 offset:23552\n"
    "ds_read_b128 a[128:131], v197 offset:8192\n"
    "ds_read_b128 a[132:135], v197 offset:9216\n"
    "ds_read_b128 a[136:139], v197 offset:10240\n"
    "ds_read_b128 a[140:143], v197 offset:11264\n"
    "ds_read_b128 a[144:147], v197 offset:12288\n"
    "ds_read_b128 a[148:151], v197 offset:13312\n"
    "s_lshl_b32 s56, s59, 8\n"
    "v_lshrrev_b32 v253, 1, v198\n"
    "v_and_b32 v253, 0xf8, v253\n"
    "v_add_u32 v253, s56, v253\n"
    "s_waitcnt lgkmcnt(4)\n"
    "ds_read_b128 a[152:155], v197 offset:14336\n"
    "ds_read_b128 a[156:159], v197 offset:15360\n"
    "ds_read_b128 a[160:163], v197 offset:24576\n"
    "ds_read_b128 a[164:167], v197 offset:25600\n"
    "ds_read_b128 a[168:171], v197 offset:26624\n"
    "ds_read_b128 a[172:175], v197 offset:27648\n"
    "ds_read_b128 a[176:179], v197 offset:28672\n"
    "ds_read_b128 a[180:183], v197 offset:29696\n"
    "ds_read_b128 a[184:187], v197 offset:30720\n"
    "ds_read_b128 a[188:191], v197 offset:31744\n"
    "s_waitcnt vmcnt(8) lgkmcnt(6)\n"
    "ds_read_b128 a[0:3], v210 offset:0\n"
    "ds_read_b128 a[4:7], v211 offset:0\n"
    "ds_read_b128 a[8:11], v212 offset:0\n"
    "ds_read_b128 a[12:15], v213 offset:0\n"
    "ds_read_b128 a[16:19], v210 offset:4096\n"
    "ds_read_b128 a[20:23], v211 offset:4096\n"
    "ds_read_b128 a[24:27], v212 offset:4096\n"
    "ds_read_b128 a[28:31], v213 offset:4096\n"
    "v_readfirstlane_b32 s62, v202\n"
    "v_readfirstlane_b32 s63, v203\n"
    "s_nop 3\n"
    "s_xor_b32 s62, s62, 0x7f4a7c15\n"
    "s_xor_b32 s63, s63, 0x9e3779b9\n"
    "s_waitcnt lgkmcnt(0)\n"
    "v_mfma_f32_16x16x32_bf16 v[128:131], a[64:67], a[0:3], 0\n"
    "v_mfma_f32_16x16x32_bf16 v[128:131], a[68:71], a[4:7], v[128:131]\n"
    "v_mfma_f32_16x16x32_bf16 v[128:131], a[72:75], a[8:11], v[128:131]\n"
    "v_mfma_f32_16x16x32_bf16 v[128:131], a[76:79], a[12:15], v[128:131]\n"
    "v_mfma_f32_16x16x32_bf16 v[132:135], a[80:83], a[0:3], 0\n"
    "v_mfma_f32_16x16x32_bf16 v[132:135], a[84:87], a[4:7], v[132:135]\n"
    "v_mfma_f32_16x16x32_bf16 v[132:135], a[88:91], a[8:11], v[132:135]\n"
    "v_mfma_f32_16x16x32_bf16 v[132:135], a[92:95], a[12:15], v[132:135]\n"
    "v_mfma_f32_16x16x32_bf16 v[136:139], a[64:67], a[16:19], 0\n"
    "v_mfma_f32_16x16x32_bf16 v[136:139], a[68:71], a[20:23], v[136:139]\n"
    "v_mfma_f32_16x16x32_bf16 v[136:139], a[72:75], a[24:27], v[136:139]\n"
    "v_mfma_f32_16x16x32_bf16 v[136:139], a[76:79], a[28:31], v[136:139]\n"
    "s_nop 3\n"
    "v_max3_f32 v204, v128, v129, v130\n"
    "v_max3_f32 v204, v204, v131, v132\n"
    "v_max3_f32 v204, v204, v133, v134\n"
    "v_max_f32 v204, v204, v135\n"
    "v_mov_b32 v205, v204\n"
    "s_nop 1\n"
    "v_permlane16_swap_b32 v204, v205\n"
    "v_mfma_f32_16x16x32_bf16 v[140:143], a[80:83], a[16:19], 0\n"
    "v_max_f32 v204, v204, v205\n"
    "v_mov_b32 v205, v204\n"
    "s_nop 1\n"
    "v_permlane32_swap_b32 v204, v205\n"
    "v_max_f32 v204, v204, v205\n"
    "v_sub_f32 v128, v128, v204\n"
    "v_sub_f32 v129, v129, v204\n"
    "v_sub_f32 v130, v130, v204\n"
    "v_sub_f32 v131, v131, v204\n"
    "v_sub_f32 v132, v132, v204\n"
    "v_mfma_f32_16x16x32_bf16 v[140:143], a[84:87], a[20:23], v[140:143]\n"
    "v_sub_f32 v133, v133, v204\n"
    "v_sub_f32 v134, v134, v204\n"
    "v_sub_f32 v135, v135, v204\n"
    "v_sub_f32 v160, 0, v204\n"
    "v_sub_f32 v161, 0, v204\n"
    "v_sub_f32 v162, 0, v204\n"
    "v_sub_f32 v163, 0, v204\n"
    "v_exp_f32 v128, v128\n"
    "v_exp_f32 v129, v129\n"
    "v_mfma_f32_16x16x32_bf16 v[140:143], a[88:91], a[24:27], v[140:143]\n"
    "v_exp_f32 v130, v130\n"
    "v_exp_f32 v131, v131\n"
    "v_exp_f32 v132, v132\n"
    "v_exp_f32 v133, v133\n"
    "v_exp_f32 v134, v134\n"
    "v_exp_f32 v135, v135\n"
    "v_mfma_f32_16x16x32_bf16 v[140:143], a[92:95], a[28:31], v[140:143]\n"
    "v_add_f32 v192, v192, v128\n"
    "v_add_f32 v192, v192, v129\n"
    "v_add_f32 v192, v192, v130\n"
    "v_add_f32 v192, v192, v131\n"
    "v_add_f32 v192, v192, v132\n"
    "v_add_f32 v192, v192, v133\n"
    "v_add_f32 v192, v192, v134\n"
    "v_add_f32 v192, v192, v135\n"
    "v_cvt_pk_bf16_f32 v176, v128, v129\n"
    "v_cvt_pk_bf16_f32 v177, v130, v131\n"
    "v_cvt_pk_bf16_f32 v178, v132, v133\n"
    "v_cvt_pk_bf16_f32 v179, v134, v135\n"
    "v_mfma_f32_16x16x32_bf16 v[128:131], a[96:99], a[0:3], v[160:163]\n"
    "global_load_dwordx2 v[202:203], v253, %[ctl] sc1\n"
    "v_max3_f32 v206, v136, v137, v138\n"
    "v_max3_f32 v206, v206, v139, v140\n"
    "v_mfma_f32_16x16x32_bf16 v[128:131], a[100:103], a[4:7], v[128:131]\n"
    "v_max3_f32 v206, v206, v141, v142\n"
    "v_max_f32 v206, v206, v143\n"
    "v_mov_b32 v207, v206\n"
    "v_mfma_f32_16x16x32_bf16 v[128:131], a[104:107], a[8:11], v[128:131]\n"
    "s_nop 1\n"
    "v_permlane16_swap_b32 v206, v207\n"
    "v_max_f32 v206, v206, v207\n"
    "v_mfma_f32_16x16x32_bf16 v[128:131], a[108:111], a[12:15], v[128:131]\n"
    "v_mov_b32 v207, v206\n"
    "s_nop 1\n"
    "v_permlane32_swap_b32 v206, v207\n"
    "v_mfma_f32_16x16x32_bf16 v[132:135], a[112:115], a[0:3], v[160:163]\n"
    "v_max_f32 v206, v206, v207\n"
    "v_sub_f32 v136, v136, v206\n"
    "v_sub_f32 v137, v137, v206\n"
    "v_mfma_f32_16x16x32_bf16 v[132:135], a[116:119], a[4:7], v[132:135]\n"
    "v_sub_f32 v138, v138, v206\n"
    "v_sub_f32 v139, v139, v206\n"
    "v_sub_f32 v140, v140, v206\n"
    "v_sub_f32 v141, v141, v206\n"
    "v_mfma_f32_16x16x32_bf16 v[132:135], a[120:123], a[8:11], v[132:135]\n"
    "v_sub_f32 v142, v142, v206\n"
    "v_sub_f32 v143, v143, v206\n"
    "v_sub_f32 v164, 0, v206\n"
    "v_mfma_f32_16x16x32_bf16 v[132:135], a[124:127], a[12:15], v[132:135]\n"
    "v_sub_f32 v165, 0, v206\n"
    "v_sub_f32 v166, 0, v206\n"
    "v_sub_f32 v167, 0, v206\n"
    "v_mfma_f32_16x16x32_bf16 v[0:3], v[176:179], a[128:131], 0\n"
    "v_exp_f32 v136, v136\n"
    "v_exp_f32 v137, v137\n"
    "v_mfma_f32_16x16x32_bf16 v[4:7], v[176:179], a[132:135], 0\n"
    "v_exp_f32 v138, v138\n"
    "v_exp_f32 v139, v139\n"
    "v_mfma_f32_16x16x32_bf16 v[8:11], v[176:179], a[136:139], 0\n"
    "v_exp_f32 v140, v140\n"
    "v_mfma_f32_16x16x32_bf16 v[12:15], v[176:179], a[140:143], 0\n"
    "v_exp_f32 v141, v141\n"
    "v_exp_f32 v142, v142\n"
    "v_mfma_f32_16x16x32_bf16 v[16:19], v[176:179], a[144:147], 0\n"
    "v_exp_f32 v143, v143\n"
    "v_add_f32 v193, v193, v136\n"
    "v_add_f32 v193, v193, v137\n"
    "v_mfma_f32_16x16x32_bf16 v[20:23], v[176:179], a[148:151], 0\n"
    "v_add_f32 v193, v193, v138\n"
    "v_add_f32 v193, v193, v139\n"
    "v_add_f32 v193, v193, v140\n"
    "v_mfma_f32_16x16x32_bf16 v[24:27], v[176:179], a[152:155], 0\n"
    "v_add_f32 v193, v193, v141\n"
    "v_add_f32 v193, v193, v142\n"
    "v_add_f32 v193, v193, v143\n"
    "v_cvt_pk_bf16_f32 v180, v136, v137\n"
    "v_mfma_f32_16x16x32_bf16 v[28:31], v[176:179], a[156:159], 0\n"
    "v_cvt_pk_bf16_f32 v181, v138, v139\n"
    "v_cvt_pk_bf16_f32 v182, v140, v141\n"
    "v_cvt_pk_bf16_f32 v183, v142, v143\n"
    "v_mfma_f32_16x16x32_bf16 v[136:139], a[96:99], a[16:19], v[164:167]\n"
    "v_exp_f32 v128, v128\n"
    "v_mfma_f32_16x16x32_bf16 v[136:139], a[100:103], a[20:23], v[136:139]\n"
    "v_add_f32 v192, v192, v128\n"
    "v_mfma_f32_16x16x32_bf16 v[136:139], a[104:107], a[24:27], v[136:139]\n"
    "v_exp_f32 v129, v129\n"
    "v_mfma_f32_16x16x32_bf16 v[136:139], a[108:111], a[28:31], v[136:139]\n"
    "v_add_f32 v192, v192, v129\n"
    "v_cvt_pk_bf16_f32 v176, v128, v129\n"
    "v_mfma_f32_16x16x32_bf16 v[140:143], a[112:115], a[16:19], v[164:167]\n"
    "v_exp_f32 v130, v130\n"
    "v_mfma_f32_16x16x32_bf16 v[140:143], a[116:119], a[20:23], v[140:143]\n"
    "v_add_f32 v192, v192, v130\n"
    "v_mfma_f32_16x16x32_bf16 v[140:143], a[120:123], a[24:27], v[140:143]\n"
    "v_exp_f32 v131, v131\n"
    "v_mfma_f32_16x16x32_bf16 v[140:143], a[124:127], a[28:31], v[140:143]\n"
    "v_add_f32 v192, v192, v131\n"
    "v_cvt_pk_bf16_f32 v177, v130, v131\n"
    "v_mfma_f32_16x16x32_bf16 v[32:35], v[180:183], a[128:131], 0\n"
    "v_exp_f32 v132, v132\n"
    "v_mfma_f32_16x16x32_bf16 v[36:39], v[180:183], a[132:135], 0\n"
    "v_add_f32 v192, v192, v132\n"
    "v_mfma_f32_16x16x32_bf16 v[40:43], v[180:183], a[136:139], 0\n"
    "v_exp_f32 v133, v133\n"
    "v_mfma_f32_16x16x32_bf16 v[44:47], v[180:183], a[140:143], 0\n"
    "v_add_f32 v192, v192, v133\n"
    "v_cvt_pk_bf16_f32 v178, v132, v133\n"
    "v_mfma_f32_16x16x32_bf16 v[48:51], v[180:183], a[144:147], 0\n"
    "v_exp_f32 v134, v134\n"
    "v_mfma_f32_16x16x32_bf16 v[52:55], v[180:183], a[148:151], 0\n"
    "v_add_f32 v192, v192, v134\n"
    "v_mfma_f32_16x16x32_bf16 v[56:59], v[180:183], a[152:155], 0\n"
    "v_exp_f32 v135, v135\n"
    "v_mfma_f32_16x16x32_bf16 v[60:63], v[180:183], a[156:159], 0\n"
    "v_add_f32 v192, v192, v135\n"
    "v_cvt_pk_bf16_f32 v179, v134, v135\n"
    "s_mov_b32 s57, 0x40000\n"
    "s_waitcnt vmcnt(0)\n"
    ".Lpolla:\n"
    "v_cmp_ne_u64 vcc, s[62:63], v[202:203]\n"
    "s_sub_u32 s57, s57, 1\n"
    "s_cmp_eq_u32 s57, 0\n"
    "s_cbranch_scc1 .Lpoll_oka\n"
    "s_and_b32 s58, vcc_lo, 0x3f\n"
    "s_cbranch_scc0 .Lpoll_oka\n"
    "s_sleep 4\n"
    "global_load_dwordx2 v[202:203], v253, %[ctl] sc1\n"
    "s_waitcnt vmcnt(0)\n"
    "s_branch .Lpolla\n"
    ".Lpoll_oka:\n"
    "ds_read_b128 a[32:35], v210 offset:32768\n"
    "ds_read_b128 a[36:39], v211 offset:32768\n"
    "ds_read_b128 a[40:43], v212 offset:32768\n"
    "ds_read_b128 a[44:47], v213 offset:32768\n"
    "ds_read_b128 a[48:51], v210 offset:36864\n"
    "ds_read_b128 a[52:55], v211 offset:36864\n"
    "ds_read_b128 a[56:59], v212 offset:36864\n"
    "ds_read_b128 a[60:63], v213 offset:36864\n"
    "s_add_u32 s69, s69, 1\n"
    "s_cmp_eq_u32 s69, s61\n"
    "s_addc_u32 s69, s69, 0\n"
    "s_lshl_b32 s71, s69, 15\n"
    "s_add_u32 s66, s38, s71\n"
    "s_addc_u32 s67, s39, 0\n"
    "s_add_u32 m0, s51, 32768\n"
    "s_nop 0\n"
    "global_load_lds_dwordx4 v198, s[66:67] offset:0 sc1\n"
    "global_load_lds_dwordx4 v198, s[66:67] offset:1024 sc1\n"
    "global_load_lds_dwordx4 v198, s[66:67] offset:2048 sc1\n"
    "global_load_lds_dwordx4 v198, s[66:67] offset:3072 sc1\n"
    "s_add_u32 m0, m0, 0x1000\n"
    "s_nop 0\n"
    "global_load_lds_dwordx4 v199, s[66:67] offset:0 sc1\n"
    "global_load_lds_dwordx4 v199, s[66:67] offset:1024 sc1\n"
    "global_load_lds_dwordx4 v199, s[66:67] offset:2048 sc1\n"
    "global_load_lds_dwordx4 v199, s[66:67] offset:3072 sc1\n"
    "s_waitcnt lgkmcnt(0)\n"
    "v_mfma_f32_16x16x32_bf16 v[144:147], a[64:67], a[32:35], 0\n"
    "s_add_u32 s69, s69, 1\n"
    "s_cmp_eq_u32 s69, s61\n"
    "s_addc_u32 s69, s69, 0\n"
    "v_mfma_f32_16x16x32_bf16 v[144:147], a[68:71], a[36:39], v[144:147]\n"
    "s_lshl_b32 s71, s69, 15\n"
    "s_add_u32 s66, s38, s71\n"
    "s_addc_u32 s67, s39, 0\n"
    "s_add_u32 m0, s51, 65536\n"
    "v_mfma_f32_16x16x32_bf16 v[144:147], a[72:75], a[40:43], v[144:147]\n"
    "s_nop 0\n"
    "global_load_lds_dwordx4 v198, s[66:67] offset:0 sc1\n"
    "v_mfma_f32_16x16x32_bf16 v[144:147], a[76:79], a[44:47], v[144:147]\n"
    "v_exp_f32 v136, v136\n"
    "v_exp_f32 v137, v137\n"
    "v_mfma_f32_16x16x32_bf16 v[148:151], a[80:83], a[32:35], 0\n"
    "v_exp_f32 v138, v138\n"
    "global_load_lds_dwordx4 v198, s[66:67] offset:1024 sc1\n"
    "v_mfma_f32_16x16x32_bf16 v[148:151], a[84:87], a[36:39], v[148:151]\n"
    "v_exp_f32 v139, v139\n"
    "v_exp_f32 v140, v140\n"
    "v_mfma_f32_16x16x32_bf16 v[148:151], a[88:91], a[40:43], v[148:151]\n"
    "global_load_lds_dwordx4 v198, s[66:67] offset:2048 sc1\n"
    "v_exp_f32 v141, v141\n"
    "v_mfma_f32_16x16x32_bf16 v[148:151], a[92:95], a[44:47], v[148:151]\n"
    "v_exp_f32 v142, v142\n"
    "global_load_lds_dwordx4 v198, s[66:67] offset:3072 sc1\n"
    "v_mfma_f32_16x16x32_bf16 v[0:3], v[176:179], a[160:163], v[0:3]\n"
    "v_exp_f32 v143, v143\n"
    "v_add_f32 v193, v193, v136\n"
    "v_mfma_f32_16x16x32_bf16 v[4:7], v[176:179], a[164:167], v[4:7]\n"
    "s_add_u32 m0, m0, 0x1000\n"
    "s_nop 0\n"
    "global_load_lds_dwordx4 v199, s[66:67] offset:0 sc1\n"
    "v_add_f32 v193, v193, v137\n"
    "v_mfma_f32_16x16x32_bf16 v[8:11], v[176:179], a[168:171], v[8:11]\n"
    "v_add_f32 v193, v193, v138\n"
    "v_add_f32 v193, v193, v139\n"
    "global_load_lds_dwordx4 v199, s[66:67] offset:1024 sc1\n"
    "v_mfma_f32_16x16x32_bf16 v[12:15], v[176:179], a[172:175], v[12:15]\n"
    "v_add_f32 v193, v193, v140\n"
    "v_add_f32 v193, v193, v141\n"
    "global_load_lds_dwordx4 v199, s[66:67] offset:2048 sc1\n"
    "v_mfma_f32_16x16x32_bf16 v[16:19], v[176:179], a[176:179], v[16:19]\n"
    "v_add_f32 v193, v193, v142\n"
    "v_add_f32 v193, v193, v143\n"
    "global_load_lds_dwordx4 v199, s[66:67] offset:3072 sc1\n"
    "v_mfma_f32_16x16x32_bf16 v[20:23], v[176:179], a[180:183], v[20:23]\n"
    "v_cvt_pk_bf16_f32 v180, v136, v137\n"
    "v_cvt_pk_bf16_f32 v181, v138, v139\n"
    "s_add_u32 s69, s69, 1\n"
    "s_cmp_eq_u32 s69, s61\n"
    "v_mfma_f32_16x16x32_bf16 v[24:27], v[176:179], a[184:187], v[24:27]\n"
    "s_addc_u32 s69, s69, 0\n"
    "s_lshl_b32 s71, s69, 15\n"
    "s_add_u32 s66, s38, s71\n"
    "v_mfma_f32_16x16x32_bf16 v[28:31], v[176:179], a[188:191], v[28:31]\n"
    "s_addc_u32 s67, s39, 0\n"
    "v_cvt_pk_bf16_f32 v182, v140, v141\n"
    "v_cvt_pk_bf16_f32 v183, v142, v143\n"
    "v_mfma_f32_16x16x32_bf16 v[152:155], a[64:67], a[48:51], 0\n"
    "buffer_store_dwordx4 a[192:195], v198, s[88:91], s50 offen offset:0 sc1\n"
    "v_max3_f32 v204, v144, v145, v146\n"
    "v_max3_f32 v204, v204, v147, v148\n"
    "v_max3_f32 v204, v204, v149, v150\n"
    "v_mfma_f32_16x16x32_bf16 v[152:155], a[68:71], a[52:55], v[152:155]\n"
    "v_max_f32 v204, v204, v151\n"
    "v_mov_b32 v205, v204\n"
    "s_nop 1\n"
    "v_mfma_f32_16x16x32_bf16 v[152:155], a[72:75], a[56:59], v[152:155]\n"
    "v_permlane16_swap_b32 v204, v205\n"
    "v_max_f32 v204, v204, v205\n"
    "v_mov_b32 v205, v204\n"
    "v_mfma_f32_16x16x32_bf16 v[152:155], a[76:79], a[60:63], v[152:155]\n"
    "s_nop 1\n"
    "v_permlane32_swap_b32 v204, v205\n"
    "buffer_store_dwordx4 a[196:199], v198, s[88:91], s50 offen offset:1024 sc1\n"
    "v_mfma_f32_16x16x32_bf16 v[156:159], a[80:83], a[48:51], 0\n"
    "v_max_f32 v204, v204, v205\n"
    "v_sub_f32 v144, v144, v204\n"
    "v_sub_f32 v145, v145, v204\n"
    "v_mfma_f32_16x16x32_bf16 v[156:159], a[84:87], a[52:55], v[156:159]\n"
    "v_sub_f32 v146, v146, v204\n"
    "v_sub_f32 v147, v147, v204\n"
    "v_sub_f32 v148, v148, v204\n"
    "v_sub_f32 v149, v149, v204\n"
    "v_mfma_f32_16x16x32_bf16 v[156:159], a[88:91], a[56:59], v[156:159]\n"
    "v_sub_f32 v150, v150, v204\n"
    "v_sub_f32 v151, v151, v204\n"
    "v_sub_f32 v168, 0, v204\n"
    "v_mfma_f32_16x16x32_bf16 v[156:159], a[92:95], a[60:63], v[156:159]\n"
    "v_sub_f32 v169, 0, v204\n"
    "buffer_store_dwordx4 a[200:203], v198, s[88:91], s50 offen offset:2048 sc1\n"
    "v_sub_f32 v170, 0, v204\n"
    "v_sub_f32 v171, 0, v204\n"
    "v_mfma_f32_16x16x32_bf16 v[32:35], v[180:183], a[160:163], v[32:35]\n"
    "v_exp_f32 v144, v144\n"
    "v_exp_f32 v145, v145\n"
    "v_mfma_f32_16x16x32_bf16 v[36:39], v[180:183], a[164:167], v[36:39]\n"
    "v_exp_f32 v146, v146\n"
    "v_exp_f32 v147, v147\n"
    "v_mfma_f32_16x16x32_bf16 v[40:43], v[180:183], a[168:171], v[40:43]\n"
    "v_exp_f32 v148, v148\n"
    "v_mfma_f32_16x16x32_bf16 v[44:47], v[180:183], a[172:175], v[44:47]\n"
    "v_exp_f32 v149, v149\n"
    "v_exp_f32 v150, v150\n"
    "v_mfma_f32_16x16x32_bf16 v[48:51], v[180:183], a[176:179], v[48:51]\n"
    "v_exp_f32 v151, v151\n"
    "v_add_f32 v194, v194, v144\n"
    "buffer_store_dwordx4 a[204:207], v198, s[88:91], s50 offen offset:3072 sc1\n"
    "v_mfma_f32_16x16x32_bf16 v[52:55], v[180:183], a[180:183], v[52:55]\n"
    "v_add_f32 v194, v194, v145\n"
    "v_add_f32 v194, v194, v146\n"
    "v_add_f32 v194, v194, v147\n"
    "v_add_f32 v194, v194, v148\n"
    "v_mfma_f32_16x16x32_bf16 v[56:59], v[180:183], a[184:187], v[56:59]\n"
    "v_add_f32 v194, v194, v149\n"
    "v_add_f32 v194, v194, v150\n"
    "v_add_f32 v194, v194, v151\n"
    "v_mfma_f32_16x16x32_bf16 v[60:63], v[180:183], a[188:191], v[60:63]\n"
    "v_cvt_pk_bf16_f32 v184, v144, v145\n"
    "v_cvt_pk_bf16_f32 v185, v146, v147\n"
    "v_cvt_pk_bf16_f32 v186, v148, v149\n"
    "v_cvt_pk_bf16_f32 v187, v150, v151\n"
    "v_mfma_f32_16x16x32_bf16 v[144:147], a[96:99], a[32:35], v[168:171]\n"
    "buffer_store_dwordx4 a[208:211], v199, s[88:91], s50 offen offset:0 sc1\n"
    "v_max3_f32 v206, v152, v153, v154\n"
    "v_max3_f32 v206, v206, v155, v156\n"
    "v_max3_f32 v206, v206, v157, v158\n"
    "v_mfma_f32_16x16x32_bf16 v[144:147], a[100:103], a[36:39], v[144:147]\n"
    "v_max_f32 v206, v206, v159\n"
    "v_mov_b32 v207, v206\n"
    "s_nop 1\n"
    "v_mfma_f32_16x16x32_bf16 v[144:147], a[104:107], a[40:43], v[144:147]\n"
    "v_permlane16_swap_b32 v206, v207\n"
    "v_max_f32 v206, v206, v207\n"
    "v_mov_b32 v207, v206\n"
    "v_mfma_f32_16x16x32_bf16 v[144:147], a[108:111], a[44:47], v[144:147]\n"
    "s_nop 1\n"
    "v_permlane32_swap_b32 v206, v207\n"
    "buffer_store_dwordx4 a[212:215], v199, s[88:91], s50 offen offset:1024 sc1\n"
    "v_mfma_f32_16x16x32_bf16 v[148:151], a[112:115], a[32:35], v[168:171]\n"
    "v_max_f32 v206, v206, v207\n"
    "v_sub_f32 v152, v152, v206\n"
    "v_sub_f32 v153, v153, v206\n"
    "v_mfma_f32_16x16x32_bf16 v[148:151], a[116:119], a[36:39], v[148:151]\n"
    "v_sub_f32 v154, v154, v206\n"
    "v_sub_f32 v155, v155, v206\n"
    "v_sub_f32 v156, v156, v206\n"
    "v_sub_f32 v157, v157, v206\n"
    "v_mfma_f32_16x16x32_bf16 v[148:151], a[120:123], a[40:43], v[148:151]\n"
    "v_sub_f32 v158, v158, v206\n"
    "v_sub_f32 v159, v159, v206\n"
    "v_sub_f32 v172, 0, v206\n"
    "v_mfma_f32_16x16x32_bf16 v[148:151], a[124:127], a[44:47], v[148:151]\n"
    "v_sub_f32 v173, 0, v206\n"
    "buffer_store_dwordx4 a[216:219], v199, s[88:91], s50 offen offset:2048 sc1\n"
    "v_sub_f32 v174, 0, v206\n"
    "v_sub_f32 v175, 0, v206\n"
    "v_mfma_f32_16x16x32_bf16 v[64:67], v[184:187], a[128:131], 0\n"
    "v_exp_f32 v152, v152\n"
    "v_exp_f32 v153, v153\n"
    "v_mfma_f32_16x16x32_bf16 v[68:71], v[184:187], a[132:135], 0\n"
    "v_exp_f32 v154, v154\n"
    "v_exp_f32 v155, v155\n"
    "v_mfma_f32_16x16x32_bf16 v[72:75], v[184:187], a[136:139], 0\n"
    "v_exp_f32 v156, v156\n"
    "v_mfma_f32_16x16x32_bf16 v[76:79], v[184:187], a[140:143], 0\n"
    "v_exp_f32 v157, v157\n"
    "v_exp_f32 v158, v158\n"
    "v_mfma_f32_16x16x32_bf16 v[80:83], v[184:187], a[144:147], 0\n"
    "v_exp_f32 v159, v159\n"
    "v_add_f32 v195, v195, v152\n"
    "buffer_store_dwordx4 a[220:223], v199, s[88:91], s50 offen offset:3072 sc1\n"
    "v_mfma_f32_16x16x32_bf16 v[84:87], v[184:187], a[148:151], 0\n"
    "v_add_f32 v195, v195, v153\n"
    "v_add_f32 v195, v195, v154\n"
    "v_add_f32 v195, v195, v155\n"
    "v_add_f32 v195, v195, v156\n"
    "v_mfma_f32_16x16x32_bf16 v[88:91], v[184:187], a[152:155], 0\n"
    "v_add_f32 v195, v195, v157\n"
    "v_add_f32 v195, v195, v158\n"
    "v_add_f32 v195, v195, v159\n"
    "v_mfma_f32_16x16x32_bf16 v[92:95], v[184:187], a[156:159], 0\n"
    "v_cvt_pk_bf16_f32 v188, v152, v153\n"
    "v_cvt_pk_bf16_f32 v189, v154, v155\n"
    "v_cvt_pk_bf16_f32 v190, v156, v157\n"
    "v_cvt_pk_bf16_f32 v191, v158, v159\n"
    "s_waitcnt vmcnt(16)\n"
    "s_barrier\n"
    "v_mfma_f32_16x16x32_bf16 v[152:155], a[96:99], a[48:51], v[172:175]\n"
    "v_exp_f32 v144, v144\n"
    "v_mfma_f32_16x16x32_bf16 v[152:155], a[100:103], a[52:55], v[152:155]\n"
    "ds_read_b128 a[64:67], v196 offset:0\n"
    "v_add_f32 v194, v194, v144\n"
    "v_mfma_f32_16x16x32_bf16 v[152:155], a[104:107], a[56:59], v[152:155]\n"
    "ds_read_b128 a[68:71], v196 offset:1024\n"
    "v_exp_f32 v145, v145\n"
    "v_mfma_f32_16x16x32_bf16 v[152:155], a[108:111], a[60:63], v[152:155]\n"
    "ds_read_b128 a[72:75], v196 offset:2048\n"
    "v_add_f32 v194, v194, v145\n"
    "v_cvt_pk_bf16_f32 v184, v144, v145\n"
    "v_mfma_f32_16x16x32_bf16 v[156:159], a[112:115], a[48:51], v[172:175]\n"
    "v_exp_f32 v146, v146\n"
    "v_mfma_f32_16x16x32_bf16 v[156:159], a[116:119], a[52:55], v[156:159]\n"
    "ds_read_b128 a[76:79], v196 offset:3072\n"
    "v_add_f32 v194, v194, v146\n"
    "v_mfma_f32_16x16x32_bf16 v[156:159], a[120:123], a[56:59], v[156:159]\n"
    "ds_read_b128 a[80:83], v196 offset:4096\n"
    "v_exp_f32 v147, v147\n"
    "v_mfma_f32_16x16x32_bf16 v[156:159], a[124:127], a[60:63], v[156:159]\n"
    "ds_read_b128 a[84:87], v196 offset:5120\n"
    "v_add_f32 v194, v194, v147\n"
    "v_cvt_pk_bf16_f32 v185, v146, v147\n"
    "v_mfma_f32_16x16x32_bf16 v[96:99], v[188:191], a[128:131], 0\n"
    "v_exp_f32 v148, v148\n"
    "v_mfma_f32_16x16x32_bf16 v[100:103], v[188:191], a[132:135], 0\n"
    "ds_read_b128 a[88:91], v196 offset:6144\n"
    "v_add_f32 v194, v194, v148\n"
    "v_mfma_f32_16x16x32_bf16 v[104:107], v[188:191], a[136:139], 0\n"
    "ds_read_b128 a[92:95], v196 offset:7168\n"
    "v_exp_f32 v149, v149\n"
    "v_mfma_f32_16x16x32_bf16 v[108:111], v[188:191], a[140:143], 0\n"
    "v_add_f32 v194, v194, v149\n"
    "v_cvt_pk_bf16_f32 v186, v148, v149\n"
    "v_mfma_f32_16x16x32_bf16 v[112:115], v[188:191], a[144:147], 0\n"
    "v_exp_f32 v150, v150\n"
    "v_mfma_f32_16x16x32_bf16 v[116:119], v[188:191], a[148:151], 0\n"
    "v_add_f32 v194, v194, v150\n"
    "v_mfma_f32_16x16x32_bf16 v[120:123], v[188:191], a[152:155], 0\n"
    "v_exp_f32 v151, v151\n"
    "v_mfma_f32_16x16x32_bf16 v[124:127], v[188:191], a[156:159], 0\n"
    "s_waitcnt lgkmcnt(0)\n"
    "v_add_f32 v194, v194, v151\n"
    "v_cvt_pk_bf16_f32 v187, v150, v151\n"
    "v_mfma_f32_16x16x32_bf16 v[128:131], a[64:67], a[0:3], v[160:163]\n"
    "s_add_u32 m0, s52, s51\n"
    "v_exp_f32 v152, v152\n"
    "v_mfma_f32_16x16x32_bf16 v[128:131], a[68:71], a[4:7], v[128:131]\n"
    "ds_read_b128 a[128:131], v196 offset:8192\n"
    "v_add_f32 v195, v195, v152\n"
    "v_mfma_f32_16x16x32_bf16 v[128:131], a[72:75], a[8:11], v[128:131]\n"
    "v_exp_f32 v153, v153\n"
    "v_mfma_f32_16x16x32_bf16 v[128:131], a[76:79], a[12:15], v[128:131]\n"
    "ds_read_b128 a[132:135], v196 offset:9216\n"
    "v_add_f32 v195, v195, v153\n"
    "v_cvt_pk_bf16_f32 v188, v152, v153\n"
    "v_mfma_f32_16x16x32_bf16 v[132:135], a[80:83], a[0:3], v[160:163]\n"
    "global_load_lds_dwordx4 v198, s[66:67] offset:0 sc1\n"
    "v_exp_f32 v154, v154\n"
    "v_mfma_f32_16x16x32_bf16 v[132:135], a[84:87], a[4:7], v[132:135]\n"
    "ds_read_b128 a[136:139], v196 offset:10240\n"
    "v_add_f32 v195, v195, v154\n"
    "v_mfma_f32_16x16x32_bf16 v[132:135], a[88:91], a[8:11], v[132:135]\n"
    "v_exp_f32 v155, v155\n"
    "v_mfma_f32_16x16x32_bf16 v[132:135], a[92:95], a[12:15], v[132:135]\n"
    "ds_read_b128 a[140:143], v196 offset:11264\n"
    "v_add_f32 v195, v195, v155\n"
    "v_cvt_pk_bf16_f32 v189, v154, v155\n"
    "v_mfma_f32_16x16x32_bf16 v[64:67], v[184:187], a[160:163], v[64:67]\n"
    "v_exp_f32 v156, v156\n"
    "v_mfma_f32_16x16x32_bf16 v[68:71], v[184:187], a[164:167], v[68:71]\n"
    "ds_read_b128 a[96:99], v196 offset:16384\n"
    "v_add_f32 v195, v195, v156\n"
    "v_mfma_f32_16x16x32_bf16 v[72:75], v[184:187], a[168:171], v[72:75]\n"
    "global_load_lds_dwordx4 v198, s[66:67] offset:1024 sc1\n"
    "v_exp_f32 v157, v157\n"
    "v_mfma_f32_16x16x32_bf16 v[76:79], v[184:187], a[172:175], v[76:79]\n"
    "v_add_f32 v195, v195, v157\n"
    "v_cvt_pk_bf16_f32 v190, v156, v157\n"
    "v_mfma_f32_16x16x32_bf16 v[80:83], v[184:187], a[176:179], v[80:83]\n"
    "v_exp_f32 v158, v158\n"
    "v_mfma_f32_16x16x32_bf16 v[84:87], v[184:187], a[180:183], v[84:87]\n"
    "ds_read_b128 a[100:103], v196 offset:17408\n"
    "v_add_f32 v195, v195, v158\n"
    "v_mfma_f32_16x16x32_bf16 v[88:91], v[184:187], a[184:187], v[88:91]\n"
    "v_exp_f32 v159, v159\n"
    "v_mfma_f32_16x16x32_bf16 v[92:95], v[184:187], a[188:191], v[92:95]\n"
    "v_add_f32 v195, v195, v159\n"
    "v_cvt_pk_bf16_f32 v191, v158, v159\n"
    "v_mfma_f32_16x16x32_bf16 v[136:139], a[64:67], a[16:19], v[164:167]\n"
    "v_exp_f32 v128, v128\n"
    "v_mfma_f32_16x16x32_bf16 v[136:139], a[68:71], a[20:23], v[136:139]\n"
    "ds_read_b128 a[144:147], v196 offset:12288\n"
    "v_add_f32 v192, v192, v128\n"
    "v_mfma_f32_16x16x32_bf16 v[136:139], a[72:75], a[24:27], v[136:139]\n"
    "s_add_u32 s52, s52, 0x8000\n"
    "v_exp_f32 v129, v129\n"
    "v_mfma_f32_16x16x32_bf16 v[136:139], a[76:79], a[28:31], v[136:139]\n"
    "s_and_b32 s52, s52, 0x1ffff\n"
    "ds_read_b128 a[148:151], v196 offset:13312\n"
    "v_add_f32 v192, v192, v129\n"
    "v_cvt_pk_bf16_f32 v176, v128, v129\n"
    "v_mfma_f32_16x16x32_bf16 v[140:143], a[80:83], a[16:19], v[164:167]\n"
    "s_add_u32 s53, s53, 0x8000\n"
    "global_load_lds_dwordx4 v198, s[66:67] offset:2048 sc1\n"
    "v_exp_f32 v130, v130\n"
    "v_mfma_f32_16x16x32_bf16 v[140:143], a[84:87], a[20:23], v[140:143]\n"
    "s_and_b32 s53, s53, 0x1ffff\n"
    "ds_read_b128 a[152:155], v196 offset:14336\n"
    "v_add_f32 v192, v192, v130\n"
    "v_mfma_f32_16x16x32_bf16 v[140:143], a[88:91], a[24:27], v[140:143]\n"
    "s_add_u32 s69, s69, 1\n"
    "v_exp_f32 v131, v131\n"
    "v_mfma_f32_16x16x32_bf16 v[140:143], a[92:95], a[28:31], v[140:143]\n"
    "s_cmp_eq_u32 s69, s61\n"
    "ds_read_b128 a[156:159], v196 offset:15360\n"
    "v_add_f32 v192, v192, v131\n"
    "v_cvt_pk_bf16_f32 v177, v130, v131\n"
    "v_mfma_f32_16x16x32_bf16 v[96:99], v[188:191], a[160:163], v[96:99]\n"
    "s_addc_u32 s69, s69, 0\n"
    "v_exp_f32 v132, v132\n"
    "v_mfma_f32_16x16x32_bf16 v[100:103], v[188:191], a[164:167], v[100:103]\n"
    "s_lshl_b32 s71, s69, 15\n"
    "ds_read_b128 a[104:107], v196 offset:18432\n"
    "v_add_f32 v192, v192, v132\n"
    "v_mfma_f32_16x16x32_bf16 v[104:107], v[188:191], a[168:171], v[104:107]\n"
    "global_load_lds_dwordx4 v198, s[66:67] offset:3072 sc1\n"
    "v_exp_f32 v133, v133\n"
    "v_mfma_f32_16x16x32_bf16 v[108:111], v[188:191], a[172:175], v[108:111]\n"
    "v_add_f32 v192, v192, v133\n"
    "v_cvt_pk_bf16_f32 v178, v132, v133\n"
    "v_mfma_f32_16x16x32_bf16 v[112:115], v[188:191], a[176:179], v[112:115]\n"
    "v_exp_f32 v134, v134\n"
    "v_mfma_f32_16x16x32_bf16 v[116:119], v[188:191], a[180:183], v[116:119]\n"
    "ds_read_b128 a[108:111], v196 offset:19456\n"
    "v_add_f32 v192, v192, v134\n"
    "v_mfma_f32_16x16x32_bf16 v[120:123], v[188:191], a[184:187], v[120:123]\n"
    "v_exp_f32 v135, v135\n"
    "v_mfma_f32_16x16x32_bf16 v[124:127], v[188:191], a[188:191], v[124:127]\n"
    "v_add_f32 v192, v192, v135\n"
    "v_cvt_pk_bf16_f32 v179, v134, v135\n"
    "v_mfma_f32_16x16x32_bf16 v[144:147], a[64:67], a[32:35], v[168:171]\n"
    "s_add_u32 m0, m0, 0x1000\n"
    "v_exp_f32 v136, v136\n"
    "v_mfma_f32_16x16x32_bf16 v[144:147], a[68:71], a[36:39], v[144:147]\n"
    "ds_read_b128 a[112:115], v196 offset:20480\n"
    "v_add_f32 v193, v193, v136\n"
    "v_mfma_f32_16x16x32_bf16 v[144:147], a[72:75], a[40:43], v[144:147]\n"
    "v_exp_f32 v137, v137\n"
    "v_mfma_f32_16x16x32_bf16 v[144:147], a[76:79], a[44:47], v[144:147]\n"
    "ds_read_b128 a[116:119], v196 offset:21504\n"
    "v_add_f32 v193, v193, v137\n"
    "v_cvt_pk_bf16_f32 v180, v136, v137\n"
    "v_mfma_f32_16x16x32_bf16 v[148:151], a[80:83], a[32:35], v[168:171]\n"
    "global_load_lds_dwordx4 v199, s[66:67] offset:0 sc1\n"
    "v_exp_f32 v138, v138\n"
    "v_mfma_f32_16x16x32_bf16 v[148:151], a[84:87], a[36:39], v[148:151]\n"
    "ds_read_b128 a[120:123], v196 offset:22528\n"
    "v_add_f32 v193, v193, v138\n"
    "v_mfma_f32_16x16x32_bf16 v[148:151], a[88:91], a[40:43], v[148:151]\n"
    "v_exp_f32 v139, v139\n"
    "v_mfma_f32_16x16x32_bf16 v[148:151], a[92:95], a[44:47], v[148:151]\n"
    "s_waitcnt lgkmcnt(5)\n"
    "ds_read_b128 a[124:127], v196 offset:23552\n"
    "v_add_f32 v193, v193, v139\n"
    "v_cvt_pk_bf16_f32 v181, v138, v139\n"
    "v_mfma_f32_16x16x32_bf16 v[0:3], v[176:179], a[128:131], v[0:3]\n"
    "v_exp_f32 v140, v140\n"
    "v_mfma_f32_16x16x32_bf16 v[4:7], v[176:179], a[132:135], v[4:7]\n"
    "ds_read_b128 a[160:163], v196 offset:24576\n"
    "v_add_f32 v193, v193, v140\n"
    "v_mfma_f32_16x16x32_bf16 v[8:11], v[176:179], a[136:139], v[8:11]\n"
    "global_load_lds_dwordx4 v199, s[66:67] offset:1024 sc1\n"
    "v_exp_f32 v141, v141\n"
    "v_mfma_f32_16x16x32_bf16 v[12:15], v[176:179], a[140:143], v[12:15]\n"
    "v_add_f32 v193, v193, v141\n"
    "v_cvt_pk_bf16_f32 v182, v140, v141\n"
    "v_mfma_f32_16x16x32_bf16 v[16:19], v[176:179], a[144:147], v[16:19]\n"
    "v_exp_f32 v142, v142\n"
    "v_mfma_f32_16x16x32_bf16 v[20:23], v[176:179], a[148:151], v[20:23]\n"
    "ds_read_b128 a[164:167], v196 offset:25600\n"
    "v_add_f32 v193, v193, v142\n"
    "v_mfma_f32_16x16x32_bf16 v[24:27], v[176:179], a[152:155], v[24:27]\n"
    "v_exp_f32 v143, v143\n"
    "v_mfma_f32_16x16x32_bf16 v[28:31], v[176:179], a[156:159], v[28:31]\n"
    "v_add_f32 v193, v193, v143\n"
    "v_cvt_pk_bf16_f32 v183, v142, v143\n"
    "v_mfma_f32_16x16x32_bf16 v[152:155], a[64:67], a[48:51], v[172:175]\n"
    "global_load_lds_dwordx4 v199, s[66:67] offset:2048 sc1\n"
    "v_exp_f32 v144, v144\n"
    "v_mfma_f32_16x16x32_bf16 v[152:155], a[68:71], a[52:55], v[152:155]\n"
    "ds_read_b128 a[168:171], v196 offset:26624\n"
    "v_add_f32 v194, v194, v144\n"
    "v_mfma_f32_16x16x32_bf16 v[152:155], a[72:75], a[56:59], v[152:155]\n"
    "ds_read_b128 a[172:175], v196 offset:27648\n"
    "v_exp_f32 v145, v145\n"
    "v_mfma_f32_16x16x32_bf16 v[152:155], a[76:79], a[60:63], v[152:155]\n"
    "ds_read_b128 a[176:179], v196 offset:28672\n"
    "v_add_f32 v194, v194, v145\n"
    "v_cvt_pk_bf16_f32 v184, v144, v145\n"
    "v_mfma_f32_16x16x32_bf16 v[156:159], a[80:83], a[48:51], v[172:175]\n"
    "global_load_lds_dwordx4 v199, s[66:67] offset:3072 sc1\n"
    "v_exp_f32 v146, v146\n"
    "v_mfma_f32_16x16x32_bf16 v[156:159], a[84:87], a[52:55], v[156:159]\n"
    "ds_read_b128 a[180:183], v196 offset:29696\n"
    "v_add_f32 v194, v194, v146\n"
    "v_mfma_f32_16x16x32_bf16 v[156:159], a[88:91], a[56:59], v[156:159]\n"
    "s_add_u32 s66, s38, s71\n"
    "v_exp_f32 v147, v147\n"
    "v_mfma_f32_16x16x32_bf16 v[156:159], a[92:95], a[60:63], v[156:159]\n"
    "s_addc_u32 s67, s39, 0\n"
    "ds_read_b128 a[184:187], v196 offset:30720\n"
    "v_add_f32 v194, v194, v147\n"
    "v_cvt_pk_bf16_f32 v185, v146, v147\n"
    "v_mfma_f32_16x16x32_bf16 v[32:35], v[180:183], a[128:131], v[32:35]\n"
    "v_exp_f32 v148, v148\n"
    "v_mfma_f32_16x16x32_bf16 v[36:39], v[180:183], a[132:135], v[36:39]\n"
    "ds_read_b128 a[188:191], v196 offset:31744\n"
    "v_add_f32 v194, v194, v148\n"
    "v_mfma_f32_16x16x32_bf16 v[40:43], v[180:183], a[136:139], v[40:43]\n"
    "v_exp_f32 v149, v149\n"
    "v_mfma_f32_16x16x32_bf16 v[44:47], v[180:183], a[140:143], v[44:47]\n"
    "v_add_f32 v194, v194, v149\n"
    "v_cvt_pk_bf16_f32 v186, v148, v149\n"
    "v_mfma_f32_16x16x32_bf16 v[48:51], v[180:183], a[144:147], v[48:51]\n"
    "v_exp_f32 v150, v150\n"
    "v_mfma_f32_16x16x32_bf16 v[52:55], v[180:183], a[148:151], v[52:55]\n"
    "v_add_f32 v194, v194, v150\n"
    "v_add_u32 v196, s53, v197\n"
    "v_mfma_f32_16x16x32_bf16 v[56:59], v[180:183], a[152:155], v[56:59]\n"
    "s_waitcnt vmcnt(8) lgkmcnt(0)\n"
    "v_exp_f32 v151, v151\n"
    "v_mfma_f32_16x16x32_bf16 v[60:63], v[180:183], a[156:159], v[60:63]\n"
    "s_barrier\n"
    "v_add_f32 v194, v194, v151\n"
    "v_cvt_pk_bf16_f32 v187, v150, v151\n"
    "s_cmp_lt_u32 s61, 6\n"
    "s_cbranch_scc1 .Lno_flag\n"
    "s_lshl_b32 s56, s59, 8\n"
    "s_lshl_b32 s57, s61, 3\n"
    "s_add_u32 s56, s56, s57\n"
    "v_mov_b32 v252, s56\n"
    "v_mov_b32 v254, s62\n"
    "v_mov_b32 v255, s63\n"
    "s_mov_b64 exec, 1\n"
    "global_store_dwordx2 v252, v[254:255], %[ctl] sc1\n"
    "s_mov_b64 exec, -1\n"
    ".Lno_flag:\n"
    "s_mov_b32 s54, 1\n"
    ".Lloop:\n"
    "v_mfma_f32_16x16x32_bf16 v[128:131], a[96:99], a[0:3], v[160:163]\n"
    "v_exp_f32 v152, v152\n"
    "v_mfma_f32_16x16x32_bf16 v[128:131], a[100:103], a[4:7], v[128:131]\n"
    "ds_read_b128 a[64:67], v196 offset:0\n"
    "v_add_f32 v195, v195, v152\n"
    "v_mfma_f32_16x16x32_bf16 v[128:131], a[104:107], a[8:11], v[128:131]\n"
    "s_add_u32 m0, s52, s51\n"
    "v_exp_f32 v153, v153\n"
    "v_mfma_f32_16x16x32_bf16 v[128:131], a[108:111], a[12:15], v[128:131]\n"
    "v_add_f32 v195, v195, v153\n"
    "v_cvt_pk_bf16_f32 v188, v152, v153\n"
    "v_mfma_f32_16x16x32_bf16 v[132:135], a[112:115], a[0:3], v[160:163]\n"
    "v_exp_f32 v154, v154\n"
    "v_mfma_f32_16x16x32_bf16 v[132:135], a[116:119], a[4:7], v[132:135]\n"
    "ds_read_b128 a[68:71], v196 offset:1024\n"
    "v_add_f32 v195, v195, v154\n"
    "v_mfma_f32_16x16x32_bf16 v[132:135], a[120:123], a[8:11], v[132:135]\n"
    "v_exp_f32 v155, v155\n"
    "v_mfma_f32_16x16x32_bf16 v[132:135], a[124:127], a[12:15], v[132:135]\n"
    "v_add_f32 v195, v195, v155\n"
    "v_cvt_pk_bf16_f32 v189, v154, v155\n"
    "v_mfma_f32_16x16x32_bf16 v[64:67], v[184:187], a[128:131], v[64:67]\n"
    "v_exp_f32 v156, v156\n"
    "v_mfma_f32_16x16x32_bf16 v[68:71], v[184:187], a[132:135], v[68:71]\n"
    "ds_read_b128 a[72:75], v196 offset:2048\n"
    "v_add_f32 v195, v195, v156\n"
    "v_mfma_f32_16x16x32_bf16 v[72:75], v[184:187], a[136:139], v[72:75]\n"
    "global_load_lds_dwordx4 v198, s[66:67] offset:0 sc1\n"
    "v_exp_f32 v157, v157\n"
    "v_mfma_f32_16x16x32_bf16 v[76:79], v[184:187], a[140:143], v[76:79]\n"
    "v_add_f32 v195, v195, v157\n"
    "v_cvt_pk_bf16_f32 v190, v156, v157\n"
    "v_mfma_f32_16x16x32_bf16 v[80:83], v[184:187], a[144:147], v[80:83]\n"
    "v_exp_f32 v158, v158\n"
    "v_mfma_f32_16x16x32_bf16 v[84:87], v[184:187], a[148:151], v[84:87]\n"
    "ds_read_b128 a[76:79], v196 offset:3072\n"
    "v_add_f32 v195, v195, v158\n"
    "v_mfma_f32_16x16x32_bf16 v[88:91], v[184:187], a[152:155], v[88:91]\n"
    "v_exp_f32 v159, v159\n"
    "v_mfma_f32_16x16x32_bf16 v[92:95], v[184:187], a[156:159], v[92:95]\n"
    "v_add_f32 v195, v195, v159\n"
    "v_cvt_pk_bf16_f32 v191, v158, v159\n"
    "v_mfma_f32_16x16x32_bf16 v[136:139], a[96:99], a[16:19], v[164:167]\n"
    "v_exp_f32 v128, v128\n"
    "v_mfma_f32_16x16x32_bf16 v[136:139], a[100:103], a[20:23], v[136:139]\n"
    "ds_read_b128 a[80:83], v196 offset:4096\n"
    "v_add_f32 v192, v192, v128\n"
    "v_mfma_f32_16x16x32_bf16 v[136:139], a[104:107], a[24:27], v[136:139]\n"
    "v_exp_f32 v129, v129\n"
    "v_mfma_f32_16x16x32_bf16 v[136:139], a[108:111], a[28:31], v[136:139]\n"
    "v_add_f32 v192, v192, v129\n"
    "v_cvt_pk_bf16_f32 v176, v128, v129\n"
    "v_mfma_f32_16x16x32_bf16 v[140:143], a[112:115], a[16:19], v[164:167]\n"
    "v_exp_f32 v130, v130\n"
    "v_mfma_f32_16x16x32_bf16 v[140:143], a[116:119], a[20:23], v[140:143]\n"
    "ds_read_b128 a[84:87], v196 offset:5120\n"
    "v_add_f32 v192, v192, v130\n"
    "v_mfma_f32_16x16x32_bf16 v[140:143], a[120:123], a[24:27], v[140:143]\n"
    "v_exp_f32 v131, v131\n"
    "v_mfma_f32_16x16x32_bf16 v[140:143], a[124:127], a[28:31], v[140:143]\n"
    "v_add_f32 v192, v192, v131\n"
    "v_cvt_pk_bf16_f32 v177, v130, v131\n"
    "v_mfma_f32_16x16x32_bf16 v[96:99], v[188:191], a[128:131], v[96:99]\n"
    "v_exp_f32 v132, v132\n"
    "v_mfma_f32_16x16x32_bf16 v[100:103], v[188:191], a[132:135], v[100:103]\n"
    "ds_read_b128 a[88:91], v196 offset:6144\n"
    "v_add_f32 v192, v192, v132\n"
    "v_mfma_f32_16x16x32_bf16 v[104:107], v[188:191], a[136:139], v[104:107]\n"
    "global_load_lds_dwordx4 v198, s[66:67] offset:1024 sc1\n"
    "v_exp_f32 v133, v133\n"
    "v_mfma_f32_16x16x32_bf16 v[108:111], v[188:191], a[140:143], v[108:111]\n"
    "v_add_f32 v192, v192, v133\n"
    "v_cvt_pk_bf16_f32 v178, v132, v133\n"
    "v_mfma_f32_16x16x32_bf16 v[112:115], v[188:191], a[144:147], v[112:115]\n"
    "v_exp_f32 v134, v134\n"
    "v_mfma_f32_16x16x32_bf16 v[116:119], v[188:191], a[148:151], v[116:119]\n"
    "ds_read_b128 a[92:95], v196 offset:7168\n"
    "v_add_f32 v192, v192, v134\n"
    "v_mfma_f32_16x16x32_bf16 v[120:123], v[188:191], a[152:155], v[120:123]\n"
    "v_exp_f32 v135, v135\n"
    "v_mfma_f32_16x16x32_bf16 v[124:127], v[188:191], a[156:159], v[124:127]\n"
    "v_add_f32 v192, v192, v135\n"
    "v_cvt_pk_bf16_f32 v179, v134, v135\n"
    "v_mfma_f32_16x16x32_bf16 v[144:147], a[96:99], a[32:35], v[168:171]\n"
    "v_exp_f32 v136, v136\n"
    "v_mfma_f32_16x16x32_bf16 v[144:147], a[100:103], a[36:39], v[144:147]\n"
    "ds_read_b128 a[128:131], v196 offset:8192\n"
    "v_add_f32 v193, v193, v136\n"
    "v_mfma_f32_16x16x32_bf16 v[144:147], a[104:107], a[40:43], v[144:147]\n"
    "v_exp_f32 v137, v137\n"
    "v_mfma_f32_16x16x32_bf16 v[144:147], a[108:111], a[44:47], v[144:147]\n"
    "v_add_f32 v193, v193, v137\n"
    "v_cvt_pk_bf16_f32 v180, v136, v137\n"
    "v_mfma_f32_16x16x32_bf16 v[148:151], a[112:115], a[32:35], v[168:171]\n"
    "v_exp_f32 v138, v138\n"
    "v_mfma_f32_16x16x32_bf16 v[148:151], a[116:119], a[36:39], v[148:151]\n"
    "ds_read_b128 a[132:135], v196 offset:9216\n"
    "v_add_f32 v193, v193, v138\n"
    "v_mfma_f32_16x16x32_bf16 v[148:151], a[120:123], a[40:43], v[148:151]\n"
    "v_exp_f32 v139, v139\n"
    "v_mfma_f32_16x16x32_bf16 v[148:151], a[124:127], a[44:47], v[148:151]\n"
    "s_waitcnt lgkmcnt(2)\n"
    "v_add_f32 v193, v193, v139\n"
    "v_cvt_pk_bf16_f32 v181, v138, v139\n"
    "v_mfma_f32_16x16x32_bf16 v[0:3], v[176:179], a[160:163], v[0:3]\n"
    "v_exp_f32 v140, v140\n"
    "v_mfma_f32_16x16x32_bf16 v[4:7], v[176:179], a[164:167], v[4:7]\n"
    "ds_read_b128 a[136:139], v196 offset:10240\n"
    "v_add_f32 v193, v193, v140\n"
    "v_mfma_f32_16x16x32_bf16 v[8:11], v[176:179], a[168:171], v[8:11]\n"
    "global_load_lds_dwordx4 v198, s[66:67] offset:2048 sc1\n"
    "v_exp_f32 v141, v141\n"
    "v_mfma_f32_16x16x32_bf16 v[12:15], v[176:179], a[172:175], v[12:15]\n"
    "v_add_f32 v193, v193, v141\n"
    "v_cvt_pk_bf16_f32 v182, v140, v141\n"
    "v_mfma_f32_16x16x32_bf16 v[16:19], v[176:179], a[176:179], v[16:19]\n"
    "v_exp_f32 v142, v142\n"
    "v_mfma_f32_16x16x32_bf16 v[20:23], v[176:179], a[180:183], v[20:23]\n"
    "ds_read_b128 a[140:143], v196 offset:11264\n"
    "v_add_f32 v193, v193, v142\n"
    "v_mfma_f32_16x16x32_bf16 v[24:27], v[176:179], a[184:187], v[24:27]\n"
    "v_exp_f32 v143, v143\n"
    "v_mfma_f32_16x16x32_bf16 v[28:31], v[176:179], a[188:191], v[28:31]\n"
    "v_add_f32 v193, v193, v143\n"
    "v_cvt_pk_bf16_f32 v183, v142, v143\n"
    "v_mfma_f32_16x16x32_bf16 v[152:155], a[96:99], a[48:51], v[172:175]\n"
    "v_exp_f32 v144, v144\n"
    "v_mfma_f32_16x16x32_bf16 v[152:155], a[100:103], a[52:55], v[152:155]\n"
    "ds_read_b128 a[144:147], v196 offset:12288\n"
    "v_add_f32 v194, v194, v144\n"
    "v_mfma_f32_16x16x32_bf16 v[152:155], a[104:107], a[56:59], v[152:155]\n"
    "v_exp_f32 v145, v145\n"
    "v_mfma_f32_16x16x32_bf16 v[152:155], a[108:111], a[60:63], v[152:155]\n"
    "v_add_f32 v194, v194, v145\n"
    "v_cvt_pk_bf16_f32 v184, v144, v145\n"
    "v_mfma_f32_16x16x32_bf16 v[156:159], a[112:115], a[48:51], v[172:175]\n"
    "v_exp_f32 v146, v146\n"
    "v_mfma_f32_16x16x32_bf16 v[156:159], a[116:119], a[52:55], v[156:159]\n"
    "ds_read_b128 a[148:151], v196 offset:13312\n"
    "v_add_f32 v194, v194, v146\n"
    "v_mfma_f32_16x16x32_bf16 v[156:159], a[120:123], a[56:59], v[156:159]\n"
    "v_exp_f32 v147, v147\n"
    "v_mfma_f32_16x16x32_bf16 v[156:159], a[124:127], a[60:63], v[156:159]\n"
    "v_add_f32 v194, v194, v147\n"
    "v_cvt_pk_bf16_f32 v185, v146, v147\n"
    "v_mfma_f32_16x16x32_bf16 v[32:35], v[180:183], a[160:163], v[32:35]\n"
    "v_exp_f32 v148, v148\n"
    "v_mfma_f32_16x16x32_bf16 v[36:39], v[180:183], a[164:167], v[36:39]\n"
    "ds_read_b128 a[152:155], v196 offset:14336\n"
    "v_add_f32 v194, v194, v148\n"
    "v_mfma_f32_16x16x32_bf16 v[40:43], v[180:183], a[168:171], v[40:43]\n"
    "global_load_lds_dwordx4 v198, s[66:67] offset:3072 sc1\n"
    "v_exp_f32 v149, v149\n"
    "v_mfma_f32_16x16x32_bf16 v[44:47], v[180:183], a[172:175], v[44:47]\n"
    "v_add_f32 v194, v194, v149\n"
    "v_cvt_pk_bf16_f32 v186, v148, v149\n"
    "v_mfma_f32_16x16x32_bf16 v[48:51], v[180:183], a[176:179], v[48:51]\n"
    "v_exp_f32 v150, v150\n"
    "v_mfma_f32_16x16x32_bf16 v[52:55], v[180:183], a[180:183], v[52:55]\n"
    "ds_read_b128 a[156:159], v196 offset:15360\n"
    "v_add_f32 v194, v194, v150\n"
    "v_mfma_f32_16x16x32_bf16 v[56:59], v[180:183], a[184:187], v[56:59]\n"
    "v_exp_f32 v151, v151\n"
    "v_mfma_f32_16x16x32_bf16 v[60:63], v[180:183], a[188:191], v[60:63]\n"
    "v_add_f32 v194, v194, v151\n"
    "v_cvt_pk_bf16_f32 v187, v150, v151\n"
    "v_mfma_f32_16x16x32_bf16 v[128:131], a[64:67], a[0:3], v[160:163]\n"
    "v_exp_f32 v152, v152\n"
    "v_mfma_f32_16x16x32_bf16 v[128:131], a[68:71], a[4:7], v[128:131]\n"
    "ds_read_b128 a[96:99], v196 offset:16384\n"
    "v_add_f32 v195, v195, v152\n"
    "v_mfma_f32_16x16x32_bf16 v[128:131], a[72:75], a[8:11], v[128:131]\n"
    "s_add_u32 m0, m0, 0x1000\n"
    "v_exp_f32 v153, v153\n"
    "v_mfma_f32_16x16x32_bf16 v[128:131], a[76:79], a[12:15], v[128:131]\n"
    "v_add_f32 v195, v195, v153\n"
    "v_cvt_pk_bf16_f32 v188, v152, v153\n"
    "v_mfma_f32_16x16x32_bf16 v[132:135], a[80:83], a[0:3], v[160:163]\n"
    "v_exp_f32 v154, v154\n"
    "v_mfma_f32_16x16x32_bf16 v[132:135], a[84:87], a[4:7], v[132:135]\n"
    "ds_read_b128 a[100:103], v196 offset:17408\n"
    "v_add_f32 v195, v195, v154\n"
    "v_mfma_f32_16x16x32_bf16 v[132:135], a[88:91], a[8:11], v[132:135]\n"
    "v_exp_f32 v155, v155\n"
    "v_mfma_f32_16x16x32_bf16 v[132:135], a[92:95], a[12:15], v[132:135]\n"
    "v_add_f32 v195, v195, v155\n"
    "v_cvt_pk_bf16_f32 v189, v154, v155\n"
    "v_mfma_f32_16x16x32_bf16 v[64:67], v[184:187], a[160:163], v[64:67]\n"
    "v_exp_f32 v156, v156\n"
    "v_mfma_f32_16x16x32_bf16 v[68:71], v[184:187], a[164:167], v[68:71]\n"
    "ds_read_b128 a[104:107], v196 offset:18432\n"
    "v_add_f32 v195, v195, v156\n"
    "v_mfma_f32_16x16x32_bf16 v[72:75], v[184:187], a[168:171], v[72:75]\n"
    "global_load_lds_dwordx4 v199, s[66:67] offset:0 sc1\n"
    "v_exp_f32 v157, v157\n"
    "v_mfma_f32_16x16x32_bf16 v[76:79], v[184:187], a[172:175], v[76:79]\n"
    "v_add_f32 v195, v195, v157\n"
    "v_cvt_pk_bf16_f32 v190, v156, v157\n"
    "v_mfma_f32_16x16x32_bf16 v[80:83], v[184:187], a[176:179], v[80:83]\n"
    "v_exp_f32 v158, v158\n"
    "v_mfma_f32_16x16x32_bf16 v[84:87], v[184:187], a[180:183], v[84:87]\n"
    "ds_read_b128 a[108:111], v196 offset:19456\n"
    "v_add_f32 v195, v195, v158\n"
    "v_mfma_f32_16x16x32_bf16 v[88:91], v[184:187], a[184:187], v[88:91]\n"
    "v_exp_f32 v159, v159\n"
    "v_mfma_f32_16x16x32_bf16 v[92:95], v[184:187], a[188:191], v[92:95]\n"
    "v_add_f32 v195, v195, v159\n"
    "v_cvt_pk_bf16_f32 v191, v158, v159\n"
    "v_mfma_f32_16x16x32_bf16 v[136:139], a[64:67], a[16:19], v[164:167]\n"
    "v_exp_f32 v128, v128\n"
    "v_mfma_f32_16x16x32_bf16 v[136:139], a[68:71], a[20:23], v[136:139]\n"
    "ds_read_b128 a[112:115], v196 offset:20480\n"
    "v_add_f32 v192, v192, v128\n"
    "v_mfma_f32_16x16x32_bf16 v[136:139], a[72:75], a[24:27], v[136:139]\n"
    "s_add_u32 s52, s52, 0x8000\n"
    "v_exp_f32 v129, v129\n"
    "v_mfma_f32_16x16x32_bf16 v[136:139], a[76:79], a[28:31], v[136:139]\n"
    "s_and_b32 s52, s52, 0x1ffff\n"
    "v_add_f32 v192, v192, v129\n"
    "v_cvt_pk_bf16_f32 v176, v128, v129\n"
    "v_mfma_f32_16x16x32_bf16 v[140:143], a[80:83], a[16:19], v[164:167]\n"
    "s_add_u32 s53, s53, 0x8000\n"
    "v_exp_f32 v130, v130\n"
    "v_mfma_f32_16x16x32_bf16 v[140:143], a[84:87], a[20:23], v[140:143]\n"
    "s_and_b32 s53, s53, 0x1ffff\n"
    "ds_read_b128 a[116:119], v196 offset:21504\n"
    "v_add_f32 v192, v192, v130\n"
    "v_mfma_f32_16x16x32_bf16 v[140:143], a[88:91], a[24:27], v[140:143]\n"
    "s_add_u32 s69, s69, 1\n"
    "v_exp_f32 v131, v131\n"
    "v_mfma_f32_16x16x32_bf16 v[140:143], a[92:95], a[28:31], v[140:143]\n"
    "s_cmp_eq_u32 s69, s61\n"
    "v_add_f32 v192, v192, v131\n"
    "v_cvt_pk_bf16_f32 v177, v130, v131\n"
    "v_mfma_f32_16x16x32_bf16 v[96:99], v[188:191], a[160:163], v[96:99]\n"
    "s_addc_u32 s69, s69, 0\n"
    "v_exp_f32 v132, v132\n"
    "v_mfma_f32_16x16x32_bf16 v[100:103], v[188:191], a[164:167], v[100:103]\n"
    "s_lshl_b32 s71, s69, 15\n"
    "ds_read_b128 a[120:123], v196 offset:22528\n"
    "v_add_f32 v192, v192, v132\n"
    "v_mfma_f32_16x16x32_bf16 v[104:107], v[188:191], a[168:171], v[104:107]\n"
    "global_load_lds_dwordx4 v199, s[66:67] offset:1024 sc1\n"
    "v_exp_f32 v133, v133\n"
    "v_mfma_f32_16x16x32_bf16 v[108:111], v[188:191], a[172:175], v[108:111]\n"
    "v_add_f32 v192, v192, v133\n"
    "v_cvt_pk_bf16_f32 v178, v132, v133\n"
    "v_mfma_f32_16x16x32_bf16 v[112:115], v[188:191], a[176:179], v[112:115]\n"
    "s_waitcnt vmcnt(6)\n"
    "v_exp_f32 v134, v134\n"
    "v_mfma_f32_16x16x32_bf16 v[116:119], v[188:191], a[180:183], v[116:119]\n"
    "s_mov_b64 exec, 1\n"
    "ds_add_u32 v200, v201\n"
    "s_mov_b64 exec, -1\n"
    "ds_read_b128 a[124:127], v196 offset:23552\n"
    "v_add_f32 v192, v192, v134\n"
    "v_mfma_f32_16x16x32_bf16 v[120:123], v[188:191], a[184:187], v[120:123]\n"
    "s_add_u32 s87, s87, 4\n"
    "v_exp_f32 v135, v135\n"
    "v_mfma_f32_16x16x32_bf16 v[124:127], v[188:191], a[188:191], v[124:127]\n"
    "v_add_f32 v192, v192, v135\n"
    "v_cvt_pk_bf16_f32 v179, v134, v135\n"
    "v_mfma_f32_16x16x32_bf16 v[144:147], a[64:67], a[32:35], v[168:171]\n"
    "v_exp_f32 v136, v136\n"
    "v_mfma_f32_16x16x32_bf16 v[144:147], a[68:71], a[36:39], v[144:147]\n"
    "ds_read_b128 a[160:163], v196 offset:24576\n"
    "v_add_f32 v193, v193, v136\n"
    "v_mfma_f32_16x16x32_bf16 v[144:147], a[72:75], a[40:43], v[144:147]\n"
    "v_exp_f32 v137, v137\n"
    "v_mfma_f32_16x16x32_bf16 v[144:147], a[76:79], a[44:47], v[144:147]\n"
    "v_add_f32 v193, v193, v137\n"
    "v_cvt_pk_bf16_f32 v180, v136, v137\n"
    "v_mfma_f32_16x16x32_bf16 v[148:151], a[80:83], a[32:35], v[168:171]\n"
    "v_exp_f32 v138, v138\n"
    "v_mfma_f32_16x16x32_bf16 v[148:151], a[84:87], a[36:39], v[148:151]\n"
    "ds_read_b128 a[164:167], v196 offset:25600\n"
    "v_add_f32 v193, v193, v138\n"
    "v_mfma_f32_16x16x32_bf16 v[148:151], a[88:91], a[40:43], v[148:151]\n"
    "v_exp_f32 v139, v139\n"
    "v_mfma_f32_16x16x32_bf16 v[148:151], a[92:95], a[44:47], v[148:151]\n"
    "s_waitcnt lgkmcnt(2)\n"
    "v_add_f32 v193, v193, v139\n"
    "v_cvt_pk_bf16_f32 v181, v138, v139\n"
    "v_mfma_f32_16x16x32_bf16 v[0:3], v[176:179], a[128:131], v[0:3]\n"
    "v_exp_f32 v140, v140\n"
    "v_mfma_f32_16x16x32_bf16 v[4:7], v[176:179], a[132:135], v[4:7]\n"
    "ds_read_b128 a[168:171], v196 offset:26624\n"
    "v_add_f32 v193, v193, v140\n"
    "v_mfma_f32_16x16x32_bf16 v[8:11], v[176:179], a[136:139], v[8:11]\n"
    "global_load_lds_dwordx4 v199, s[66:67] offset:2048 sc1\n"
    "v_exp_f32 v141, v141\n"
    "v_mfma_f32_16x16x32_bf16 v[12:15], v[176:179], a[140:143], v[12:15]\n"
    "v_add_f32 v193, v193, v141\n"
    "v_cvt_pk_bf16_f32 v182, v140, v141\n"
    "v_mfma_f32_16x16x32_bf16 v[16:19], v[176:179], a[144:147], v[16:19]\n"
    "v_exp_f32 v142, v142\n"
    "v_mfma_f32_16x16x32_bf16 v[20:23], v[176:179], a[148:151], v[20:23]\n"
    "ds_read_b128 a[172:175], v196 offset:27648\n"
    "v_add_f32 v193, v193, v142\n"
    "v_mfma_f32_16x16x32_bf16 v[24:27], v[176:179], a[152:155], v[24:27]\n"
    "v_exp_f32 v143, v143\n"
    "v_mfma_f32_16x16x32_bf16 v[28:31], v[176:179], a[156:159], v[28:31]\n"
    "v_add_f32 v193, v193, v143\n"
    "v_cvt_pk_bf16_f32 v183, v142, v143\n"
    "v_mfma_f32_16x16x32_bf16 v[152:155], a[64:67], a[48:51], v[172:175]\n"
    "v_exp_f32 v144, v144\n"
    "v_mfma_f32_16x16x32_bf16 v[152:155], a[68:71], a[52:55], v[152:155]\n"
    "ds_read_b128 a[176:179], v196 offset:28672\n"
    "v_add_f32 v194, v194, v144\n"
    "v_mfma_f32_16x16x32_bf16 v[152:155], a[72:75], a[56:59], v[152:155]\n"
    "global_load_lds_dwordx4 v199, s[66:67] offset:3072 sc1\n"
    "v_exp_f32 v145, v145\n"
    "v_mfma_f32_16x16x32_bf16 v[152:155], a[76:79], a[60:63], v[152:155]\n"
    "ds_read_b128 a[180:183], v196 offset:29696\n"
    "v_add_f32 v194, v194, v145\n"
    "v_cvt_pk_bf16_f32 v184, v144, v145\n"
    "v_mfma_f32_16x16x32_bf16 v[156:159], a[80:83], a[48:51], v[172:175]\n"
    "v_exp_f32 v146, v146\n"
    "v_mfma_f32_16x16x32_bf16 v[156:159], a[84:87], a[52:55], v[156:159]\n"
    "ds_read_b128 a[184:187], v196 offset:30720\n"
    "v_add_f32 v194, v194, v146\n"
    "v_mfma_f32_16x16x32_bf16 v[156:159], a[88:91], a[56:59], v[156:159]\n"
    "s_add_u32 s66, s38, s71\n"
    "v_exp_f32 v147, v147\n"
    "v_mfma_f32_16x16x32_bf16 v[156:159], a[92:95], a[60:63], v[156:159]\n"
    "s_addc_u32 s67, s39, 0\n"
    "ds_read_b32 v252, v200\n"
    "v_add_f32 v194, v194, v147\n"
    "v_cvt_pk_bf16_f32 v185, v146, v147\n"
    "v_mfma_f32_16x16x32_bf16 v[32:35], v[180:183], a[128:131], v[32:35]\n"
    "s_sub_u32 s54, s54, 1\n"
    "v_exp_f32 v148, v148\n"
    "v_mfma_f32_16x16x32_bf16 v[36:39], v[180:183], a[132:135], v[36:39]\n"
    "s_cmp_lg_u32 s54, 0\n"
    "ds_read_b128 a[188:191], v196 offset:31744\n"
    "v_add_f32 v194, v194, v148\n"
    "v_mfma_f32_16x16x32_bf16 v[40:43], v[180:183], a[136:139], v[40:43]\n"
    "v_exp_f32 v149, v149\n"
    "v_mfma_f32_16x16x32_bf16 v[44:47], v[180:183], a[140:143], v[44:47]\n"
    "v_add_f32 v194, v194, v149\n"
    "v_cvt_pk_bf16_f32 v186, v148, v149\n"
    "v_mfma_f32_16x16x32_bf16 v[48:51], v[180:183], a[144:147], v[48:51]\n"
    "v_exp_f32 v150, v150\n"
    "v_mfma_f32_16x16x32_bf16 v[52:55], v[180:183], a[148:151], v[52:55]\n"
    "s_waitcnt lgkmcnt(1)\n"
    "v_add_f32 v194, v194, v150\n"
    "v_cmp_gt_u32 vcc, s87, v252\n"
    "v_mfma_f32_16x16x32_bf16 v[56:59], v[180:183], a[152:155], v[56:59]\n"
    "v_exp_f32 v151, v151\n"
    "v_add_u32 v196, s53, v197\n"
    "v_mfma_f32_16x16x32_bf16 v[60:63], v[180:183], a[156:159], v[60:63]\n"
    "v_add_f32 v194, v194, v151\n"
    "v_cvt_pk_bf16_f32 v187, v150, v151\n"
    "s_cbranch_vccnz .Lslow0\n"
    ".Lback0:\n"
    "s_cbranch_scc1 .Lloop\n"
    "s_add_u32 s70, s70, 1\n"
    "s_mov_b32 s54, 1\n"
    "s_cmp_eq_u32 s70, 3\n"
    "s_cbranch_scc1 .Lloop_done\n"
    "s_cmp_eq_u32 s70, 2\n"
    "s_cbranch_scc1 .Ld3\n"
    "global_load_dwordx2 v[202:203], v253, %[ctl] sc1\n"
    "s_branch .Lloop\n"
    ".Ld3:\n"
    "s_mov_b32 s57, 0x40000\n"
    "s_waitcnt vmcnt(8)\n"
    ".Lpollb:\n"
    "v_cmp_ne_u64 vcc, s[62:63], v[202:203]\n"
    "s_sub_u32 s57, s57, 1\n"
    "s_cmp_eq_u32 s57, 0\n"
    "s_cbranch_scc1 .Lpoll_okb\n"
    "s_and_b32 s58, vcc_lo, -1\n"
    "s_cbranch_scc0 .Lpoll_okb\n"
    "s_sleep 4\n"
    "global_load_dwordx2 v[202:203], v253, %[ctl] sc1\n"
    "s_waitcnt vmcnt(0)\n"
    "s_branch .Lpollb\n"
    ".Lpoll_okb:\n"
    "s_mov_b32 s54, 26\n"
    "s_branch .Lloop\n"
    ".Lloop_done:\n"
    "v_mfma_f32_16x16x32_bf16 v[128:131], a[96:99], a[0:3], v[160:163]\n"
    "v_exp_f32 v152, v152\n"
    "v_mfma_f32_16x16x32_bf16 v[128:131], a[100:103], a[4:7], v[128:131]\n"
    "ds_read_b128 a[64:67], v196 offset:0\n"
    "v_add_f32 v195, v195, v152\n"
    "v_mfma_f32_16x16x32_bf16 v[128:131], a[104:107], a[8:11], v[128:131]\n"
    "v_exp_f32 v153, v153\n"
    "v_mfma_f32_16x16x32_bf16 v[128:131], a[108:111], a[12:15], v[128:131]\n"
    "v_add_f32 v195, v195, v153\n"
    "v_cvt_pk_bf16_f32 v188, v152, v153\n"
    "v_mfma_f32_16x16x32_bf16 v[132:135], a[112:115], a[0:3], v[160:163]\n"
    "v_exp_f32 v154, v154\n"
    "v_mfma_f32_16x16x32_bf16 v[132:135], a[116:119], a[4:7], v[132:135]\n"
    "ds_read_b128 a[68:71], v196 offset:1024\n"
    "v_add_f32 v195, v195, v154\n"
    "v_mfma_f32_16x16x32_bf16 v[132:135], a[120:123], a[8:11], v[132:135]\n"
    "v_exp_f32 v155, v155\n"
    "v_mfma_f32_16x16x32_bf16 v[132:135], a[124:127], a[12:15], v[132:135]\n"
    "v_add_f32 v195, v195, v155\n"
    "v_cvt_pk_bf16_f32 v189, v154, v155\n"
    "v_mfma_f32_16x16x32_bf16 v[64:67], v[184:187], a[128:131], v[64:67]\n"
    "v_exp_f32 v156, v156\n"
    "v_mfma_f32_16x16x32_bf16 v[68:71], v[184:187], a[132:135], v[68:71]\n"
    "ds_read_b128 a[72:75], v196 offset:2048\n"
    "v_add_f32 v195, v195, v156\n"
    "v_mfma_f32_16x16x32_bf16 v[72:75], v[184:187], a[136:139], v[72:75]\n"
    "v_exp_f32 v157, v157\n"
    "v_mfma_f32_16x16x32_bf16 v[76:79], v[184:187], a[140:143], v[76:79]\n"
    "v_add_f32 v195, v195, v157\n"
    "v_cvt_pk_bf16_f32 v190, v156, v157\n"
    "v_mfma_f32_16x16x32_bf16 v[80:83], v[184:187], a[144:147], v[80:83]\n"
    "v_exp_f32 v158, v158\n"
    "v_mfma_f32_16x16x32_bf16 v[84:87], v[184:187], a[148:151], v[84:87]\n"
    "ds_read_b128 a[76:79], v196 offset:3072\n"
    "v_add_f32 v195, v195, v158\n"
    "v_mfma_f32_16x16x32_bf16 v[88:91], v[184:187], a[152:155], v[88:91]\n"
    "v_exp_f32 v159, v159\n"
    "v_mfma_f32_16x16x32_bf16 v[92:95], v[184:187], a[156:159], v[92:95]\n"
    "v_add_f32 v195, v195, v159\n"
    "v_cvt_pk_bf16_f32 v191, v158, v159\n"
    "v_mfma_f32_16x16x32_bf16 v[136:139], a[96:99], a[16:19], v[164:167]\n"
    "v_exp_f32 v128, v128\n"
    "v_mfma_f32_16x16x32_bf16 v[136:139], a[100:103], a[20:23], v[136:139]\n"
    "ds_read_b128 a[80:83], v196 offset:4096\n"
    "v_add_f32 v192, v192, v128\n"
    "v_mfma_f32_16x16x32_bf16 v[136:139], a[104:107], a[24:27], v[136:139]\n"
    "v_exp_f32 v129, v129\n"
    "v_mfma_f32_16x16x32_bf16 v[136:139], a[108:111], a[28:31], v[136:139]\n"
    "v_add_f32 v192, v192, v129\n"
    "v_cvt_pk_bf16_f32 v176, v128, v129\n"
    "v_mfma_f32_16x16x32_bf16 v[140:143], a[112:115], a[16:19], v[164:167]\n"
    "v_exp_f32 v130, v130\n"
    "v_mfma_f32_16x16x32_bf16 v[140:143], a[116:119], a[20:23], v[140:143]\n"
    "ds_read_b128 a[84:87], v196 offset:5120\n"
    "v_add_f32 v192, v192, v130\n"
    "v_mfma_f32_16x16x32_bf16 v[140:143], a[120:123], a[24:27], v[140:143]\n"
    "v_exp_f32 v131, v131\n"
    "v_mfma_f32_16x16x32_bf16 v[140:143], a[124:127], a[28:31], v[140:143]\n"
    "v_add_f32 v192, v192, v131\n"
    "v_cvt_pk_bf16_f32 v177, v130, v131\n"
    "v_mfma_f32_16x16x32_bf16 v[96:99], v[188:191], a[128:131], v[96:99]\n"
    "v_exp_f32 v132, v132\n"
    "v_mfma_f32_16x16x32_bf16 v[100:103], v[188:191], a[132:135], v[100:103]\n"
    "ds_read_b128 a[88:91], v196 offset:6144\n"
    "v_add_f32 v192, v192, v132\n"
    "v_mfma_f32_16x16x32_bf16 v[104:107], v[188:191], a[136:139], v[104:107]\n"
    "v_exp_f32 v133, v133\n"
    "v_mfma_f32_16x16x32_bf16 v[108:111], v[188:191], a[140:143], v[108:111]\n"
    "v_add_f32 v192, v192, v133\n"
    "v_cvt_pk_bf16_f32 v178, v132, v133\n"
    "v_mfma_f32_16x16x32_bf16 v[112:115], v[188:191], a[144:147], v[112:115]\n"
    "v_exp_f32 v134, v134\n"
    "v_mfma_f32_16x16x32_bf16 v[116:119], v[188:191], a[148:151], v[116:119]\n"
    "ds_read_b128 a[92:95], v196 offset:7168\n"
    "v_add_f32 v192, v192, v134\n"
    "v_mfma_f32_16x16x32_bf16 v[120:123], v[188:191], a[152:155], v[120:123]\n"
    "v_exp_f32 v135, v135\n"
    "v_mfma_f32_16x16x32_bf16 v[124:127], v[188:191], a[156:159], v[124:127]\n"
    "v_add_f32 v192, v192, v135\n"
    "v_cvt_pk_bf16_f32 v179, v134, v135\n"
    "v_mfma_f32_16x16x32_bf16 v[144:147], a[96:99], a[32:35], v[168:171]\n"
    "v_exp_f32 v136, v136\n"
    "v_mfma_f32_16x16x32_bf16 v[144:147], a[100:103], a[36:39], v[144:147]\n"
    "ds_read_b128 a[128:131], v196 offset:8192\n"
    "v_add_f32 v193, v193, v136\n"
    "v_mfma_f32_16x16x32_bf16 v[144:147], a[104:107], a[40:43], v[144:147]\n"
    "v_exp_f32 v137, v137\n"
    "v_mfma_f32_16x16x32_bf16 v[144:147], a[108:111], a[44:47], v[144:147]\n"
    "v_add_f32 v193, v193, v137\n"
    "v_cvt_pk_bf16_f32 v180, v136, v137\n"
    "v_mfma_f32_16x16x32_bf16 v[148:151], a[112:115], a[32:35], v[168:171]\n"
    "v_exp_f32 v138, v138\n"
    "v_mfma_f32_16x16x32_bf16 v[148:151], a[116:119], a[36:39], v[148:151]\n"
    "ds_read_b128 a[132:135], v196 offset:9216\n"
    "v_add_f32 v193, v193, v138\n"
    "v_mfma_f32_16x16x32_bf16 v[148:151], a[120:123], a[40:43], v[148:151]\n"
    "v_exp_f32 v139, v139\n"
    "v_mfma_f32_16x16x32_bf16 v[148:151], a[124:127], a[44:47], v[148:151]\n"
    "s_waitcnt lgkmcnt(2)\n"
    "v_add_f32 v193, v193, v139\n"
    "v_cvt_pk_bf16_f32 v181, v138, v139\n"
    "v_mfma_f32_16x16x32_bf16 v[0:3], v[176:179], a[160:163], v[0:3]\n"
    "v_exp_f32 v140, v140\n"
    "v_mfma_f32_16x16x32_bf16 v[4:7], v[176:179], a[164:167], v[4:7]\n"
    "ds_read_b128 a[136:139], v196 offset:10240\n"
    "v_add_f32 v193, v193, v140\n"
    "v_mfma_f32_16x16x32_bf16 v[8:11], v[176:179], a[168:171], v[8:11]\n"
    "v_exp_f32 v141, v141\n"
    "v_mfma_f32_16x16x32_bf16 v[12:15], v[176:179], a[172:175], v[12:15]\n"
    "v_add_f32 v193, v193, v141\n"
    "v_cvt_pk_bf16_f32 v182, v140, v141\n"
    "v_mfma_f32_16x16x32_bf16 v[16:19], v[176:179], a[176:179], v[16:19]\n"
    "v_exp_f32 v142, v142\n"
    "v_mfma_f32_16x16x32_bf16 v[20:23], v[176:179], a[180:183], v[20:23]\n"
    "ds_read_b128 a[140:143], v196 offset:11264\n"
    "v_add_f32 v193, v193, v142\n"
    "v_mfma_f32_16x16x32_bf16 v[24:27], v[176:179], a[184:187], v[24:27]\n"
    "v_exp_f32 v143, v143\n"
    "v_mfma_f32_16x16x32_bf16 v[28:31], v[176:179], a[188:191], v[28:31]\n"
    "v_add_f32 v193, v193, v143\n"
    "v_cvt_pk_bf16_f32 v183, v142, v143\n"
    "v_mfma_f32_16x16x32_bf16 v[152:155], a[96:99], a[48:51], v[172:175]\n"
    "v_exp_f32 v144, v144\n"
    "v_mfma_f32_16x16x32_bf16 v[152:155], a[100:103], a[52:55], v[152:155]\n"
    "ds_read_b128 a[144:147], v196 offset:12288\n"
    "v_add_f32 v194, v194, v144\n"
    "v_mfma_f32_16x16x32_bf16 v[152:155], a[104:107], a[56:59], v[152:155]\n"
    "v_exp_f32 v145, v145\n"
    "v_mfma_f32_16x16x32_bf16 v[152:155], a[108:111], a[60:63], v[152:155]\n"
    "v_add_f32 v194, v194, v145\n"
    "v_cvt_pk_bf16_f32 v184, v144, v145\n"
    "v_mfma_f32_16x16x32_bf16 v[156:159], a[112:115], a[48:51], v[172:175]\n"
    "v_exp_f32 v146, v146\n"
    "v_mfma_f32_16x16x32_bf16 v[156:159], a[116:119], a[52:55], v[156:159]\n"
    "ds_read_b128 a[148:151], v196 offset:13312\n"
    "v_add_f32 v194, v194, v146\n"
    "v_mfma_f32_16x16x32_bf16 v[156:159], a[120:123], a[56:59], v[156:159]\n"
    "v_exp_f32 v147, v147\n"
    "v_mfma_f32_16x16x32_bf16 v[156:159], a[124:127], a[60:63], v[156:159]\n"
    "v_add_f32 v194, v194, v147\n"
    "v_cvt_pk_bf16_f32 v185, v146, v147\n"
    "v_mfma_f32_16x16x32_bf16 v[32:35], v[180:183], a[160:163], v[32:35]\n"
    "v_exp_f32 v148, v148\n"
    "v_mfma_f32_16x16x32_bf16 v[36:39], v[180:183], a[164:167], v[36:39]\n"
    "ds_read_b128 a[152:155], v196 offset:14336\n"
    "v_add_f32 v194, v194, v148\n"
    "v_mfma_f32_16x16x32_bf16 v[40:43], v[180:183], a[168:171], v[40:43]\n"
    "v_exp_f32 v149, v149\n"
    "v_mfma_f32_16x16x32_bf16 v[44:47], v[180:183], a[172:175], v[44:47]\n"
    "v_add_f32 v194, v194, v149\n"
    "v_cvt_pk_bf16_f32 v186, v148, v149\n"
    "v_mfma_f32_16x16x32_bf16 v[48:51], v[180:183], a[176:179], v[48:51]\n"
    "v_exp_f32 v150, v150\n"
    "v_mfma_f32_16x16x32_bf16 v[52:55], v[180:183], a[180:183], v[52:55]\n"
    "ds_read_b128 a[156:159], v196 offset:15360\n"
    "v_add_f32 v194, v194, v150\n"
    "v_mfma_f32_16x16x32_bf16 v[56:59], v[180:183], a[184:187], v[56:59]\n"
    "v_exp_f32 v151, v151\n"
    "v_mfma_f32_16x16x32_bf16 v[60:63], v[180:183], a[188:191], v[60:63]\n"
    "v_add_f32 v194, v194, v151\n"
    "v_cvt_pk_bf16_f32 v187, v150, v151\n"
    "v_mfma_f32_16x16x32_bf16 v[128:131], a[64:67], a[0:3], v[160:163]\n"
    "v_exp_f32 v152, v152\n"
    "v_mfma_f32_16x16x32_bf16 v[128:131], a[68:71], a[4:7], v[128:131]\n"
    "ds_read_b128 a[96:99], v196 offset:16384\n"
    "v_add_f32 v195, v195, v152\n"
    "v_mfma_f32_16x16x32_bf16 v[128:131], a[72:75], a[8:11], v[128:131]\n"
    "v_exp_f32 v153, v153\n"
    "v_mfma_f32_16x16x32_bf16 v[128:131], a[76:79], a[12:15], v[128:131]\n"
    "v_add_f32 v195, v195, v153\n"
    "v_cvt_pk_bf16_f32 v188, v152, v153\n"
    "v_mfma_f32_16x16x32_bf16 v[132:135], a[80:83], a[0:3], v[160:163]\n"
    "v_exp_f32 v154, v154\n"
    "v_mfma_f32_16x16x32_bf16 v[132:135], a[84:87], a[4:7], v[132:135]\n"
    "ds_read_b128 a[100:103], v196 offset:17408\n"
    "v_add_f32 v195, v195, v154\n"
    "v_mfma_f32_16x16x32_bf16 v[132:135], a[88:91], a[8:11], v[132:135]\n"
    "v_exp_f32 v155, v155\n"
    "v_mfma_f32_16x16x32_bf16 v[132:135], a[92:95], a[12:15], v[132:135]\n"
    "v_add_f32 v195, v195, v155\n"
    "v_cvt_pk_bf16_f32 v189, v154, v155\n"
    "v_mfma_f32_16x16x32_bf16 v[64:67], v[184:187], a[160:163], v[64:67]\n"
    "v_exp_f32 v156, v156\n"
    "v_mfma_f32_16x16x32_bf16 v[68:71], v[184:187], a[164:167], v[68:71]\n"
    "ds_read_b128 a[104:107], v196 offset:18432\n"
    "v_add_f32 v195, v195, v156\n"
    "v_mfma_f32_16x16x32_bf16 v[72:75], v[184:187], a[168:171], v[72:75]\n"
    "v_exp_f32 v157, v157\n"
    "v_mfma_f32_16x16x32_bf16 v[76:79], v[184:187], a[172:175], v[76:79]\n"
    "v_add_f32 v195, v195, v157\n"
    "v_cvt_pk_bf16_f32 v190, v156, v157\n"
    "v_mfma_f32_16x16x32_bf16 v[80:83], v[184:187], a[176:179], v[80:83]\n"
    "v_exp_f32 v158, v158\n"
    "v_mfma_f32_16x16x32_bf16 v[84:87], v[184:187], a[180:183], v[84:87]\n"
    "ds_read_b128 a[108:111], v196 offset:19456\n"
    "v_add_f32 v195, v195, v158\n"
    "v_mfma_f32_16x16x32_bf16 v[88:91], v[184:187], a[184:187], v[88:91]\n"
    "v_exp_f32 v159, v159\n"
    "v_mfma_f32_16x16x32_bf16 v[92:95], v[184:187], a[188:191], v[92:95]\n"
    "v_add_f32 v195, v195, v159\n"
    "v_cvt_pk_bf16_f32 v191, v158, v159\n"
    "v_mfma_f32_16x16x32_bf16 v[136:139], a[64:67], a[16:19], v[164:167]\n"
    "v_exp_f32 v128, v128\n"
    "v_mfma_f32_16x16x32_bf16 v[136:139], a[68:71], a[20:23], v[136:139]\n"
    "ds_read_b128 a[112:115], v196 offset:20480\n"
    "v_add_f32 v192, v192, v128\n"
    "v_mfma_f32_16x16x32_bf16 v[136:139], a[72:75], a[24:27], v[136:139]\n"
    "s_add_u32 s52, s52, 0x8000\n"
    "v_exp_f32 v129, v129\n"
    "v_mfma_f32_16x16x32_bf16 v[136:139], a[76:79], a[28:31], v[136:139]\n"
    "s_and_b32 s52, s52, 0x1ffff\n"
    "v_add_f32 v192, v192, v129\n"
    "v_cvt_pk_bf16_f32 v176, v128, v129\n"
    "v_mfma_f32_16x16x32_bf16 v[140:143], a[80:83], a[16:19], v[164:167]\n"
    "s_add_u32 s53, s53, 0x8000\n"
    "v_exp_f32 v130, v130\n"
    "v_mfma_f32_16x16x32_bf16 v[140:143], a[84:87], a[20:23], v[140:143]\n"
    "s_and_b32 s53, s53, 0x1ffff\n"
    "ds_read_b128 a[116:119], v196 offset:21504\n"
    "v_add_f32 v192, v192, v130\n"
    "v_mfma_f32_16x16x32_bf16 v[140:143], a[88:91], a[24:27], v[140:143]\n"
    "v_exp_f32 v131, v131\n"
    "v_mfma_f32_16x16x32_bf16 v[140:143], a[92:95], a[28:31], v[140:143]\n"
    "v_add_f32 v192, v192, v131\n"
    "v_cvt_pk_bf16_f32 v177, v130, v131\n"
    "v_mfma_f32_16x16x32_bf16 v[96:99], v[188:191], a[160:163], v[96:99]\n"
    "v_exp_f32 v132, v132\n"
    "v_mfma_f32_16x16x32_bf16 v[100:103], v[188:191], a[164:167], v[100:103]\n"
    "ds_read_b128 a[120:123], v196 offset:22528\n"
    "v_add_f32 v192, v192, v132\n"
    "v_mfma_f32_16x16x32_bf16 v[104:107], v[188:191], a[168:171], v[104:107]\n"
    "v_exp_f32 v133, v133\n"
    "v_mfma_f32_16x16x32_bf16 v[108:111], v[188:191], a[172:175], v[108:111]\n"
    "v_add_f32 v192, v192, v133\n"
    "v_cvt_pk_bf16_f32 v178, v132, v133\n"
    "v_mfma_f32_16x16x32_bf16 v[112:115], v[188:191], a[176:179], v[112:115]\n"
    "s_waitcnt vmcnt(0)\n"
    "v_exp_f32 v134, v134\n"
    "v_mfma_f32_16x16x32_bf16 v[116:119], v[188:191], a[180:183], v[116:119]\n"
    "s_mov_b64 exec, 1\n"
    "ds_add_u32 v200, v201\n"
    "s_mov_b64 exec, -1\n"
    "ds_read_b128 a[124:127], v196 offset:23552\n"
    "v_add_f32 v192, v192, v134\n"
    "v_mfma_f32_16x16x32_bf16 v[120:123], v[188:191], a[184:187], v[120:123]\n"
    "s_add_u32 s87, s87, 4\n"
    "v_exp_f32 v135, v135\n"
    "v_mfma_f32_16x16x32_bf16 v[124:127], v[188:191], a[188:191], v[124:127]\n"
    "v_add_f32 v192, v192, v135\n"
    "v_cvt_pk_bf16_f32 v179, v134, v135\n"
    "v_mfma_f32_16x16x32_bf16 v[144:147], a[64:67], a[32:35], v[168:171]\n"
    "v_exp_f32 v136, v136\n"
    "v_mfma_f32_16x16x32_bf16 v[144:147], a[68:71], a[36:39], v[144:147]\n"
    "ds_read_b128 a[160:163], v196 offset:24576\n"
    "v_add_f32 v193, v193, v136\n"
    "v_mfma_f32_16x16x32_bf16 v[144:147], a[72:75], a[40:43], v[144:147]\n"
    "v_exp_f32 v137, v137\n"
    "v_mfma_f32_16x16x32_bf16 v[144:147], a[76:79], a[44:47], v[144:147]\n"
    "v_add_f32 v193, v193, v137\n"
    "v_cvt_pk_bf16_f32 v180, v136, v137\n"
    "v_mfma_f32_16x16x32_bf16 v[148:151], a[80:83], a[32:35], v[168:171]\n"
    "v_exp_f32 v138, v138\n"
    "v_mfma_f32_16x16x32_bf16 v[148:151], a[84:87], a[36:39], v[148:151]\n"
    "ds_read_b128 a[164:167], v196 offset:25600\n"
    "v_add_f32 v193, v193, v138\n"
    "v_mfma_f32_16x16x32_bf16 v[148:151], a[88:91], a[40:43], v[148:151]\n"
    "v_exp_f32 v139, v139\n"
    "v_mfma_f32_16x16x32_bf16 v[148:151], a[92:95], a[44:47], v[148:151]\n"
    "s_waitcnt lgkmcnt(2)\n"
    "v_add_f32 v193, v193, v139\n"
    "v_cvt_pk_bf16_f32 v181, v138, v139\n"
    "v_mfma_f32_16x16x32_bf16 v[0:3], v[176:179], a[128:131], v[0:3]\n"
    "v_exp_f32 v140, v140\n"
    "v_mfma_f32_16x16x32_bf16 v[4:7], v[176:179], a[132:135], v[4:7]\n"
    "ds_read_b128 a[168:171], v196 offset:26624\n"
    "v_add_f32 v193, v193, v140\n"
    "v_mfma_f32_16x16x32_bf16 v[8:11], v[176:179], a[136:139], v[8:11]\n"
    "v_exp_f32 v141, v141\n"
    "v_mfma_f32_16x16x32_bf16 v[12:15], v[176:179], a[140:143], v[12:15]\n"
    "v_add_f32 v193, v193, v141\n"
    "v_cvt_pk_bf16_f32 v182, v140, v141\n"
    "v_mfma_f32_16x16x32_bf16 v[16:19], v[176:179], a[144:147], v[16:19]\n"
    "v_exp_f32 v142, v142\n"
    "v_mfma_f32_16x16x32_bf16 v[20:23], v[176:179], a[148:151], v[20:23]\n"
    "ds_read_b128 a[172:175], v196 offset:27648\n"
    "v_add_f32 v193, v193, v142\n"
    "v_mfma_f32_16x16x32_bf16 v[24:27], v[176:179], a[152:155], v[24:27]\n"
    "v_exp_f32 v143, v143\n"
    "v_mfma_f32_16x16x32_bf16 v[28:31], v[176:179], a[156:159], v[28:31]\n"
    "v_add_f32 v193, v193, v143\n"
    "v_cvt_pk_bf16_f32 v183, v142, v143\n"
    "v_mfma_f32_16x16x32_bf16 v[152:155], a[64:67], a[48:51], v[172:175]\n"
    "v_exp_f32 v144, v144\n"
    "v_mfma_f32_16x16x32_bf16 v[152:155], a[68:71], a[52:55], v[152:155]\n"
    "ds_read_b128 a[176:179], v196 offset:28672\n"
    "v_add_f32 v194, v194, v144\n"
    "v_mfma_f32_16x16x32_bf16 v[152:155], a[72:75], a[56:59], v[152:155]\n"
    "v_exp_f32 v145, v145\n"
    "v_mfma_f32_16x16x32_bf16 v[152:155], a[76:79], a[60:63], v[152:155]\n"
    "ds_read_b128 a[180:183], v196 offset:29696\n"
    "v_add_f32 v194, v194, v145\n"
    "v_cvt_pk_bf16_f32 v184, v144, v145\n"
    "v_mfma_f32_16x16x32_bf16 v[156:159], a[80:83], a[48:51], v[172:175]\n"
    "v_exp_f32 v146, v146\n"
    "v_mfma_f32_16x16x32_bf16 v[156:159], a[84:87], a[52:55], v[156:159]\n"
    "ds_read_b128 a[184:187], v196 offset:30720\n"
    "v_add_f32 v194, v194, v146\n"
    "v_mfma_f32_16x16x32_bf16 v[156:159], a[88:91], a[56:59], v[156:159]\n"
    "v_exp_f32 v147, v147\n"
    "v_mfma_f32_16x16x32_bf16 v[156:159], a[92:95], a[60:63], v[156:159]\n"
    "ds_read_b32 v252, v200\n"
    "v_add_f32 v194, v194, v147\n"
    "v_cvt_pk_bf16_f32 v185, v146, v147\n"
    "v_mfma_f32_16x16x32_bf16 v[32:35], v[180:183], a[128:131], v[32:35]\n"
    "v_exp_f32 v148, v148\n"
    "v_mfma_f32_16x16x32_bf16 v[36:39], v[180:183], a[132:135], v[36:39]\n"
    "ds_read_b128 a[188:191], v196 offset:31744\n"
    "v_add_f32 v194, v194, v148\n"
    "v_mfma_f32_16x16x32_bf16 v[40:43], v[180:183], a[136:139], v[40:43]\n"
    "v_exp_f32 v149, v149\n"
    "v_mfma_f32_16x16x32_bf16 v[44:47], v[180:183], a[140:143], v[44:47]\n"
    "v_add_f32 v194, v194, v149\n"
    "v_cvt_pk_bf16_f32 v186, v148, v149\n"
    "v_mfma_f32_16x16x32_bf16 v[48:51], v[180:183], a[144:147], v[48:51]\n"
    "v_exp_f32 v150, v150\n"
    "v_mfma_f32_16x16x32_bf16 v[52:55], v[180:183], a[148:151], v[52:55]\n"
    "s_waitcnt lgkmcnt(1)\n"
    "v_add_f32 v194, v194, v150\n"
    "v_cmp_gt_u32 vcc, s87, v252\n"
    "v_mfma_f32_16x16x32_bf16 v[56:59], v[180:183], a[152:155], v[56:59]\n"
    "v_exp_f32 v151, v151\n"
    "v_add_u32 v196, s53, v197\n"
    "v_mfma_f32_16x16x32_bf16 v[60:63], v[180:183], a[156:159], v[60:63]\n"
    "v_add_f32 v194, v194, v151\n"
    "v_cvt_pk_bf16_f32 v187, v150, v151\n"
    "s_cbranch_vccnz .Lslow1\n"
    ".Lback1:\n"
    "v_mfma_f32_16x16x32_bf16 v[128:131], a[96:99], a[0:3], v[160:163]\n"
    "v_exp_f32 v152, v152\n"
    "v_mfma_f32_16x16x32_bf16 v[128:131], a[100:103], a[4:7], v[128:131]\n"
    "ds_read_b128 a[64:67], v196 offset:0\n"
    "v_add_f32 v195, v195, v152\n"
    "v_mfma_f32_16x16x32_bf16 v[128:131], a[104:107], a[8:11], v[128:131]\n"
    "v_exp_f32 v153, v153\n"
    "v_mfma_f32_16x16x32_bf16 v[128:131], a[108:111], a[12:15], v[128:131]\n"
    "v_add_f32 v195, v195, v153\n"
    "v_cvt_pk_bf16_f32 v188, v152, v153\n"
    "v_mfma_f32_16x16x32_bf16 v[132:135], a[112:115], a[0:3], v[160:163]\n"
    "v_exp_f32 v154, v154\n"
    "v_mfma_f32_16x16x32_bf16 v[132:135], a[116:119], a[4:7], v[132:135]\n"
    "ds_read_b128 a[68:71], v196 offset:1024\n"
    "v_add_f32 v195, v195, v154\n"
    "v_mfma_f32_16x16x32_bf16 v[132:135], a[120:123], a[8:11], v[132:135]\n"
    "v_exp_f32 v155, v155\n"
    "v_mfma_f32_16x16x32_bf16 v[132:135], a[124:127], a[12:15], v[132:135]\n"
    "v_add_f32 v195, v195, v155\n"
    "v_cvt_pk_bf16_f32 v189, v154, v155\n"
    "v_mfma_f32_16x16x32_bf16 v[64:67], v[184:187], a[128:131], v[64:67]\n"
    "v_exp_f32 v156, v156\n"
    "v_mfma_f32_16x16x32_bf16 v[68:71], v[184:187], a[132:135], v[68:71]\n"
    "ds_read_b128 a[72:75], v196 offset:2048\n"
    "v_add_f32 v195, v195, v156\n"
    "v_mfma_f32_16x16x32_bf16 v[72:75], v[184:187], a[136:139], v[72:75]\n"
    "v_exp_f32 v157, v157\n"
    "v_mfma_f32_16x16x32_bf16 v[76:79], v[184:187], a[140:143], v[76:79]\n"
    "v_add_f32 v195, v195, v157\n"
    "v_cvt_pk_bf16_f32 v190, v156, v157\n"
    "v_mfma_f32_16x16x32_bf16 v[80:83], v[184:187], a[144:147], v[80:83]\n"
    "v_exp_f32 v158, v158\n"
    "v_mfma_f32_16x16x32_bf16 v[84:87], v[184:187], a[148:151], v[84:87]\n"
    "ds_read_b128 a[76:79], v196 offset:3072\n"
    "v_add_f32 v195, v195, v158\n"
    "v_mfma_f32_16x16x32_bf16 v[88:91], v[184:187], a[152:155], v[88:91]\n"
    "v_exp_f32 v159, v159\n"
    "v_mfma_f32_16x16x32_bf16 v[92:95], v[184:187], a[156:159], v[92:95]\n"
    "v_add_f32 v195, v195, v159\n"
    "v_cvt_pk_bf16_f32 v191, v158, v159\n"
    "v_mfma_f32_16x16x32_bf16 v[136:139], a[96:99], a[16:19], v[164:167]\n"
    "v_exp_f32 v128, v128\n"
    "v_mfma_f32_16x16x32_bf16 v[136:139], a[100:103], a[20:23], v[136:139]\n"
    "ds_read_b128 a[80:83], v196 offset:4096\n"
    "v_add_f32 v192, v192, v128\n"
    "v_mfma_f32_16x16x32_bf16 v[136:139], a[104:107], a[24:27], v[136:139]\n"
    "v_exp_f32 v129, v129\n"
    "v_mfma_f32_16x16x32_bf16 v[136:139], a[108:111], a[28:31], v[136:139]\n"
    "v_add_f32 v192, v192, v129\n"
    "v_cvt_pk_bf16_f32 v176, v128, v129\n"
    "v_mfma_f32_16x16x32_bf16 v[140:143], a[112:115], a[16:19], v[164:167]\n"
    "v_exp_f32 v130, v130\n"
    "v_mfma_f32_16x16x32_bf16 v[140:143], a[116:119], a[20:23], v[140:143]\n"
    "ds_read_b128 a[84:87], v196 offset:5120\n"
    "v_add_f32 v192, v192, v130\n"
    "v_mfma_f32_16x16x32_bf16 v[140:143], a[120:123], a[24:27], v[140:143]\n"
    "v_exp_f32 v131, v131\n"
    "v_mfma_f32_16x16x32_bf16 v[140:143], a[124:127], a[28:31], v[140:143]\n"
    "v_add_f32 v192, v192, v131\n"
    "v_cvt_pk_bf16_f32 v177, v130, v131\n"
    "v_mfma_f32_16x16x32_bf16 v[96:99], v[188:191], a[128:131], v[96:99]\n"
    "v_exp_f32 v132, v132\n"
    "v_mfma_f32_16x16x32_bf16 v[100:103], v[188:191], a[132:135], v[100:103]\n"
    "ds_read_b128 a[88:91], v196 offset:6144\n"
    "v_add_f32 v192, v192, v132\n"
    "v_mfma_f32_16x16x32_bf16 v[104:107], v[188:191], a[136:139], v[104:107]\n"
    "v_exp_f32 v133, v133\n"
    "v_mfma_f32_16x16x32_bf16 v[108:111], v[188:191], a[140:143], v[108:111]\n"
    "v_add_f32 v192, v192, v133\n"
    "v_cvt_pk_bf16_f32 v178, v132, v133\n"
    "v_mfma_f32_16x16x32_bf16 v[112:115], v[188:191], a[144:147], v[112:115]\n"
    "v_exp_f32 v134, v134\n"
    "v_mfma_f32_16x16x32_bf16 v[116:119], v[188:191], a[148:151], v[116:119]\n"
    "ds_read_b128 a[92:95], v196 offset:7168\n"
    "v_add_f32 v192, v192, v134\n"
    "v_mfma_f32_16x16x32_bf16 v[120:123], v[188:191], a[152:155], v[120:123]\n"
    "v_exp_f32 v135, v135\n"
    "v_mfma_f32_16x16x32_bf16 v[124:127], v[188:191], a[156:159], v[124:127]\n"
    "v_add_f32 v192, v192, v135\n"
    "v_cvt_pk_bf16_f32 v179, v134, v135\n"
    "v_mfma_f32_16x16x32_bf16 v[144:147], a[96:99], a[32:35], v[168:171]\n"
    "v_exp_f32 v136, v136\n"
    "v_mfma_f32_16x16x32_bf16 v[144:147], a[100:103], a[36:39], v[144:147]\n"
    "ds_read_b128 a[128:131], v196 offset:8192\n"
    "v_add_f32 v193, v193, v136\n"
    "v_mfma_f32_16x16x32_bf16 v[144:147], a[104:107], a[40:43], v[144:147]\n"
    "v_exp_f32 v137, v137\n"
    "v_mfma_f32_16x16x32_bf16 v[144:147], a[108:111], a[44:47], v[144:147]\n"
    "v_add_f32 v193, v193, v137\n"
    "v_cvt_pk_bf16_f32 v180, v136, v137\n"
    "v_mfma_f32_16x16x32_bf16 v[148:151], a[112:115], a[32:35], v[168:171]\n"
    "v_exp_f32 v138, v138\n"
    "v_mfma_f32_16x16x32_bf16 v[148:151], a[116:119], a[36:39], v[148:151]\n"
    "ds_read_b128 a[132:135], v196 offset:9216\n"
    "v_add_f32 v193, v193, v138\n"
    "v_mfma_f32_16x16x32_bf16 v[148:151], a[120:123], a[40:43], v[148:151]\n"
    "v_exp_f32 v139, v139\n"
    "v_mfma_f32_16x16x32_bf16 v[148:151], a[124:127], a[44:47], v[148:151]\n"
    "s_waitcnt lgkmcnt(2)\n"
    "v_add_f32 v193, v193, v139\n"
    "v_cvt_pk_bf16_f32 v181, v138, v139\n"
    "v_mfma_f32_16x16x32_bf16 v[0:3], v[176:179], a[160:163], v[0:3]\n"
    "v_exp_f32 v140, v140\n"
    "v_mfma_f32_16x16x32_bf16 v[4:7], v[176:179], a[164:167], v[4:7]\n"
    "ds_read_b128 a[136:139], v196 offset:10240\n"
    "v_add_f32 v193, v193, v140\n"
    "v_mfma_f32_16x16x32_bf16 v[8:11], v[176:179], a[168:171], v[8:11]\n"
    "v_exp_f32 v141, v141\n"
    "v_mfma_f32_16x16x32_bf16 v[12:15], v[176:179], a[172:175], v[12:15]\n"
    "v_add_f32 v193, v193, v141\n"
    "v_cvt_pk_bf16_f32 v182, v140, v141\n"
    "v_mfma_f32_16x16x32_bf16 v[16:19], v[176:179], a[176:179], v[16:19]\n"
    "v_exp_f32 v142, v142\n"
    "v_mfma_f32_16x16x32_bf16 v[20:23], v[176:179], a[180:183], v[20:23]\n"
    "ds_read_b128 a[140:143], v196 offset:11264\n"
    "v_add_f32 v193, v193, v142\n"
    "v_mfma_f32_16x16x32_bf16 v[24:27], v[176:179], a[184:187], v[24:27]\n"
    "v_exp_f32 v143, v143\n"
    "v_mfma_f32_16x16x32_bf16 v[28:31], v[176:179], a[188:191], v[28:31]\n"
    "v_add_f32 v193, v193, v143\n"
    "v_cvt_pk_bf16_f32 v183, v142, v143\n"
    "v_mfma_f32_16x16x32_bf16 v[152:155], a[96:99], a[48:51], v[172:175]\n"
    "v_exp_f32 v144, v144\n"
    "v_mfma_f32_16x16x32_bf16 v[152:155], a[100:103], a[52:55], v[152:155]\n"
    "ds_read_b128 a[144:147], v196 offset:12288\n"
    "v_add_f32 v194, v194, v144\n"
    "v_mfma_f32_16x16x32_bf16 v[152:155], a[104:107], a[56:59], v[152:155]\n"
    "v_exp_f32 v145, v145\n"
    "v_mfma_f32_16x16x32_bf16 v[152:155], a[108:111], a[60:63], v[152:155]\n"
    "v_add_f32 v194, v194, v145\n"
    "v_cvt_pk_bf16_f32 v184, v144, v145\n"
    "v_mfma_f32_16x16x32_bf16 v[156:159], a[112:115], a[48:51], v[172:175]\n"
    "v_exp_f32 v146, v146\n"
    "v_mfma_f32_16x16x32_bf16 v[156:159], a[116:119], a[52:55], v[156:159]\n"
    "ds_read_b128 a[148:151], v196 offset:13312\n"
    "v_add_f32 v194, v194, v146\n"
    "v_mfma_f32_16x16x32_bf16 v[156:159], a[120:123], a[56:59], v[156:159]\n"
    "v_exp_f32 v147, v147\n"
    "v_mfma_f32_16x16x32_bf16 v[156:159], a[124:127], a[60:63], v[156:159]\n"
    "v_add_f32 v194, v194, v147\n"
    "v_cvt_pk_bf16_f32 v185, v146, v147\n"
    "v_mfma_f32_16x16x32_bf16 v[32:35], v[180:183], a[160:163], v[32:35]\n"
    "v_exp_f32 v148, v148\n"
    "v_mfma_f32_16x16x32_bf16 v[36:39], v[180:183], a[164:167], v[36:39]\n"
    "ds_read_b128 a[152:155], v196 offset:14336\n"
    "v_add_f32 v194, v194, v148\n"
    "v_mfma_f32_16x16x32_bf16 v[40:43], v[180:183], a[168:171], v[40:43]\n"
    "v_exp_f32 v149, v149\n"
    "v_mfma_f32_16x16x32_bf16 v[44:47], v[180:183], a[172:175], v[44:47]\n"
    "v_add_f32 v194, v194, v149\n"
    "v_cvt_pk_bf16_f32 v186, v148, v149\n"
    "v_mfma_f32_16x16x32_bf16 v[48:51], v[180:183], a[176:179], v[48:51]\n"
    "v_exp_f32 v150, v150\n"
    "v_mfma_f32_16x16x32_bf16 v[52:55], v[180:183], a[180:183], v[52:55]\n"
    "ds_read_b128 a[156:159], v196 offset:15360\n"
    "v_add_f32 v194, v194, v150\n"
    "v_mfma_f32_16x16x32_bf16 v[56:59], v[180:183], a[184:187], v[56:59]\n"
    "v_exp_f32 v151, v151\n"
    "v_mfma_f32_16x16x32_bf16 v[60:63], v[180:183], a[188:191], v[60:63]\n"
    "v_add_f32 v194, v194, v151\n"
    "v_cvt_pk_bf16_f32 v187, v150, v151\n"
    "v_mfma_f32_16x16x32_bf16 v[128:131], a[64:67], a[0:3], v[160:163]\n"
    "v_exp_f32 v152, v152\n"
    "v_mfma_f32_16x16x32_bf16 v[128:131], a[68:71], a[4:7], v[128:131]\n"
    "ds_read_b128 a[96:99], v196 offset:16384\n"
    "v_add_f32 v195, v195, v152\n"
    "v_mfma_f32_16x16x32_bf16 v[128:131], a[72:75], a[8:11], v[128:131]\n"
    "v_exp_f32 v153, v153\n"
    "v_mfma_f32_16x16x32_bf16 v[128:131], a[76:79], a[12:15], v[128:131]\n"
    "ds_read_b128 a[100:103], v196 offset:17408\n"
    "v_add_f32 v195, v195, v153\n"
    "v_cvt_pk_bf16_f32 v188, v152, v153\n"
    "v_mfma_f32_16x16x32_bf16 v[132:135], a[80:83], a[0:3], v[160:163]\n"
    "v_exp_f32 v154, v154\n"
    "v_mfma_f32_16x16x32_bf16 v[132:135], a[84:87], a[4:7], v[132:135]\n"
    "ds_read_b128 a[104:107], v196 offset:18432\n"
    "v_add_f32 v195, v195, v154\n"
    "v_mfma_f32_16x16x32_bf16 v[132:135], a[88:91], a[8:11], v[132:135]\n"
    "v_exp_f32 v155, v155\n"
    "v_mfma_f32_16x16x32_bf16 v[132:135], a[92:95], a[12:15], v[132:135]\n"
    "ds_read_b128 a[108:111], v196 offset:19456\n"
    "v_add_f32 v195, v195, v155\n"
    "v_cvt_pk_bf16_f32 v189, v154, v155\n"
    "v_mfma_f32_16x16x32_bf16 v[64:67], v[184:187], a[160:163], v[64:67]\n"
    "v_exp_f32 v156, v156\n"
    "v_mfma_f32_16x16x32_bf16 v[68:71], v[184:187], a[164:167], v[68:71]\n"
    "ds_read_b128 a[112:115], v196 offset:20480\n"
    "v_add_f32 v195, v195, v156\n"
    "v_mfma_f32_16x16x32_bf16 v[72:75], v[184:187], a[168:171], v[72:75]\n"
    "v_exp_f32 v157, v157\n"
    "v_mfma_f32_16x16x32_bf16 v[76:79], v[184:187], a[172:175], v[76:79]\n"
    "v_add_f32 v195, v195, v157\n"
    "v_cvt_pk_bf16_f32 v190, v156, v157\n"
    "v_mfma_f32_16x16x32_bf16 v[80:83], v[184:187], a[176:179], v[80:83]\n"
    "v_exp_f32 v158, v158\n"
    "v_mfma_f32_16x16x32_bf16 v[84:87], v[184:187], a[180:183], v[84:87]\n"
    "ds_read_b128 a[116:119], v196 offset:21504\n"
    "v_add_f32 v195, v195, v158\n"
    "v_mfma_f32_16x16x32_bf16 v[88:91], v[184:187], a[184:187], v[88:91]\n"
    "v_exp_f32 v159, v159\n"
    "v_mfma_f32_16x16x32_bf16 v[92:95], v[184:187], a[188:191], v[92:95]\n"
    "v_add_f32 v195, v195, v159\n"
    "v_cvt_pk_bf16_f32 v191, v158, v159\n"
    "v_mfma_f32_16x16x32_bf16 v[136:139], a[64:67], a[16:19], v[164:167]\n"
    "v_exp_f32 v128, v128\n"
    "v_mfma_f32_16x16x32_bf16 v[136:139], a[68:71], a[20:23], v[136:139]\n"
    "ds_read_b128 a[120:123], v196 offset:22528\n"
    "v_add_f32 v192, v192, v128\n"
    "v_mfma_f32_16x16x32_bf16 v[136:139], a[72:75], a[24:27], v[136:139]\n"
    "v_exp_f32 v129, v129\n"
    "v_mfma_f32_16x16x32_bf16 v[136:139], a[76:79], a[28:31], v[136:139]\n"
    "v_add_f32 v192, v192, v129\n"
    "v_cvt_pk_bf16_f32 v176, v128, v129\n"
    "v_mfma_f32_16x16x32_bf16 v[140:143], a[80:83], a[16:19], v[164:167]\n"
    "v_exp_f32 v130, v130\n"
    "v_mfma_f32_16x16x32_bf16 v[140:143], a[84:87], a[20:23], v[140:143]\n"
    "ds_read_b128 a[124:127], v196 offset:23552\n"
    "v_add_f32 v192, v192, v130\n"
    "v_mfma_f32_16x16x32_bf16 v[140:143], a[88:91], a[24:27], v[140:143]\n"
    "v_exp_f32 v131, v131\n"
    "v_mfma_f32_16x16x32_bf16 v[140:143], a[92:95], a[28:31], v[140:143]\n"
    "v_add_f32 v192, v192, v131\n"
    "v_cvt_pk_bf16_f32 v177, v130, v131\n"
    "v_mfma_f32_16x16x32_bf16 v[96:99], v[188:191], a[160:163], v[96:99]\n"
    "v_exp_f32 v132, v132\n"
    "v_mfma_f32_16x16x32_bf16 v[100:103], v[188:191], a[164:167], v[100:103]\n"
    "v_add_f32 v192, v192, v132\n"
    "v_mfma_f32_16x16x32_bf16 v[104:107], v[188:191], a[168:171], v[104:107]\n"
    "v_exp_f32 v133, v133\n"
    "v_mfma_f32_16x16x32_bf16 v[108:111], v[188:191], a[172:175], v[108:111]\n"
    "v_add_f32 v192, v192, v133\n"
    "v_cvt_pk_bf16_f32 v178, v132, v133\n"
    "v_mfma_f32_16x16x32_bf16 v[112:115], v[188:191], a[176:179], v[112:115]\n"
    "v_exp_f32 v134, v134\n"
    "v_mfma_f32_16x16x32_bf16 v[116:119], v[188:191], a[180:183], v[116:119]\n"
    "v_add_f32 v192, v192, v134\n"
    "v_mfma_f32_16x16x32_bf16 v[120:123], v[188:191], a[184:187], v[120:123]\n"
    "v_exp_f32 v135, v135\n"
    "v_mfma_f32_16x16x32_bf16 v[124:127], v[188:191], a[188:191], v[124:127]\n"
    "s_waitcnt lgkmcnt(0)\n"
    "v_add_f32 v192, v192, v135\n"
    "v_cvt_pk_bf16_f32 v179, v134, v135\n"
    "v_mfma_f32_16x16x32_bf16 v[128:131], a[96:99], a[0:3], v[160:163]\n"
    "v_exp_f32 v136, v136\n"
    "v_mfma_f32_16x16x32_bf16 v[128:131], a[100:103], a[4:7], v[128:131]\n"
    "ds_read_b128 a[160:163], v196 offset:24576\n"
    "v_add_f32 v193, v193, v136\n"
    "v_mfma_f32_16x16x32_bf16 v[128:131], a[104:107], a[8:11], v[128:131]\n"
    "v_exp_f32 v137, v137\n"
    "v_mfma_f32_16x16x32_bf16 v[128:131], a[108:111], a[12:15], v[128:131]\n"
    "v_add_f32 v193, v193, v137\n"
    "v_cvt_pk_bf16_f32 v180, v136, v137\n"
    "v_mfma_f32_16x16x32_bf16 v[132:135], a[112:115], a[0:3], v[160:163]\n"
    "v_exp_f32 v138, v138\n"
    "v_mfma_f32_16x16x32_bf16 v[132:135], a[116:119], a[4:7], v[132:135]\n"
    "ds_read_b128 a[164:167], v196 offset:25600\n"
    "v_add_f32 v193, v193, v138\n"
    "v_mfma_f32_16x16x32_bf16 v[132:135], a[120:123], a[8:11], v[132:135]\n"
    "v_exp_f32 v139, v139\n"
    "v_mfma_f32_16x16x32_bf16 v[132:135], a[124:127], a[12:15], v[132:135]\n"
    "v_add_f32 v193, v193, v139\n"
    "v_cvt_pk_bf16_f32 v181, v138, v139\n"
    "v_mfma_f32_16x16x32_bf16 v[0:3], v[176:179], a[128:131], v[0:3]\n"
    "v_exp_f32 v140, v140\n"
    "v_mfma_f32_16x16x32_bf16 v[4:7], v[176:179], a[132:135], v[4:7]\n"
    "ds_read_b128 a[168:171], v196 offset:26624\n"
    "v_add_f32 v193, v193, v140\n"
    "v_mfma_f32_16x16x32_bf16 v[8:11], v[176:179], a[136:139], v[8:11]\n"
    "v_exp_f32 v141, v141\n"
    "v_mfma_f32_16x16x32_bf16 v[12:15], v[176:179], a[140:143], v[12:15]\n"
    "v_add_f32 v193, v193, v141\n"
    "v_cvt_pk_bf16_f32 v182, v140, v141\n"
    "v_mfma_f32_16x16x32_bf16 v[16:19], v[176:179], a[144:147], v[16:19]\n"
    "v_exp_f32 v142, v142\n"
    "v_mfma_f32_16x16x32_bf16 v[20:23], v[176:179], a[148:151], v[20:23]\n"
    "ds_read_b128 a[172:175], v196 offset:27648\n"
    "v_add_f32 v193, v193, v142\n"
    "v_mfma_f32_16x16x32_bf16 v[24:27], v[176:179], a[152:155], v[24:27]\n"
    "v_exp_f32 v143, v143\n"
    "v_mfma_f32_16x16x32_bf16 v[28:31], v[176:179], a[156:159], v[28:31]\n"
    "v_add_f32 v193, v193, v143\n"
    "v_cvt_pk_bf16_f32 v183, v142, v143\n"
    "v_mfma_f32_16x16x32_bf16 v[136:139], a[96:99], a[16:19], v[164:167]\n"
    "v_exp_f32 v128, v128\n"
    "v_mfma_f32_16x16x32_bf16 v[136:139], a[100:103], a[20:23], v[136:139]\n"
    "ds_read_b128 a[176:179], v196 offset:28672\n"
    "v_add_f32 v192, v192, v128\n"
    "v_mfma_f32_16x16x32_bf16 v[136:139], a[104:107], a[24:27], v[136:139]\n"
    "v_exp_f32 v129, v129\n"
    "v_mfma_f32_16x16x32_bf16 v[136:139], a[108:111], a[28:31], v[136:139]\n"
    "v_add_f32 v192, v192, v129\n"
    "v_cvt_pk_bf16_f32 v176, v128, v129\n"
    "v_mfma_f32_16x16x32_bf16 v[140:143], a[112:115], a[16:19], v[164:167]\n"
    "v_exp_f32 v130, v130\n"
    "v_mfma_f32_16x16x32_bf16 v[140:143], a[116:119], a[20:23], v[140:143]\n"
    "ds_read_b128 a[180:183], v196 offset:29696\n"
    "v_add_f32 v192, v192, v130\n"
    "v_mfma_f32_16x16x32_bf16 v[140:143], a[120:123], a[24:27], v[140:143]\n"
    "v_exp_f32 v131, v131\n"
    "v_mfma_f32_16x16x32_bf16 v[140:143], a[124:127], a[28:31], v[140:143]\n"
    "v_add_f32 v192, v192, v131\n"
    "v_cvt_pk_bf16_f32 v177, v130, v131\n"
    "v_mfma_f32_16x16x32_bf16 v[32:35], v[180:183], a[128:131], v[32:35]\n"
    "v_exp_f32 v132, v132\n"
    "v_mfma_f32_16x16x32_bf16 v[36:39], v[180:183], a[132:135], v[36:39]\n"
    "ds_read_b128 a[184:187], v196 offset:30720\n"
    "v_add_f32 v192, v192, v132\n"
    "v_mfma_f32_16x16x32_bf16 v[40:43], v[180:183], a[136:139], v[40:43]\n"
    "v_exp_f32 v133, v133\n"
    "v_mfma_f32_16x16x32_bf16 v[44:47], v[180:183], a[140:143], v[44:47]\n"
    "v_add_f32 v192, v192, v133\n"
    "v_cvt_pk_bf16_f32 v178, v132, v133\n"
    "v_mfma_f32_16x16x32_bf16 v[48:51], v[180:183], a[144:147], v[48:51]\n"
    "v_exp_f32 v134, v134\n"
    "v_mfma_f32_16x16x32_bf16 v[52:55], v[180:183], a[148:151], v[52:55]\n"
    "ds_read_b128 a[188:191], v196 offset:31744\n"
    "v_add_f32 v192, v192, v134\n"
    "v_mfma_f32_16x16x32_bf16 v[56:59], v[180:183], a[152:155], v[56:59]\n"
    "v_exp_f32 v135, v135\n"
    "v_mfma_f32_16x16x32_bf16 v[60:63], v[180:183], a[156:159], v[60:63]\n"
    "v_add_f32 v192, v192, v135\n"
    "v_cvt_pk_bf16_f32 v179, v134, v135\n"
    "v_mfma_f32_16x16x32_bf16 v[144:147], a[64:67], a[32:35], v[168:171]\n"
    "s_waitcnt lgkmcnt(0)\n"
    "v_exp_f32 v136, v136\n"
    "v_mfma_f32_16x16x32_bf16 v[144:147], a[68:71], a[36:39], v[144:147]\n"
    "v_exp_f32 v137, v137\n"
    "s_lshl_b32 s79, s59, 8\n"
    "v_mfma_f32_16x16x32_bf16 v[144:147], a[72:75], a[40:43], v[144:147]\n"
    "v_exp_f32 v138, v138\n"
    "v_mfma_f32_16x16x32_bf16 v[144:147], a[76:79], a[44:47], v[144:147]\n"
    "s_add_u32 s79, s79, s60\n"
    "v_exp_f32 v139, v139\n"
    "v_mfma_f32_16x16x32_bf16 v[148:151], a[80:83], a[32:35], v[168:171]\n"
    "s_add_u32 s79, s79, 0x20400\n"
    "v_exp_f32 v140, v140\n"
    "v_mfma_f32_16x16x32_bf16 v[148:151], a[84:87], a[36:39], v[148:151]\n"
    "v_bfe_u32 v224, v198, 4, 4\n"
    "v_exp_f32 v141, v141\n"
    "v_mfma_f32_16x16x32_bf16 v[148:151], a[88:91], a[40:43], v[148:151]\n"
    "v_lshlrev_b32 v224, 2, v224\n"
    "v_exp_f32 v142, v142\n"
    "v_mfma_f32_16x16x32_bf16 v[148:151], a[92:95], a[44:47], v[148:151]\n"
    "v_exp_f32 v143, v143\n"
    "v_add_u32 v224, s79, v224\n"
    "v_mfma_f32_16x16x32_bf16 v[0:3], v[176:179], a[160:163], v[0:3]\n"
    "v_add_f32 v193, v193, v136\n"
    "v_lshrrev_b32 v225, 8, v198\n"
    "v_mfma_f32_16x16x32_bf16 v[4:7], v[176:179], a[164:167], v[4:7]\n"
    "v_add_f32 v193, v193, v137\n"
    "v_lshlrev_b32 v226, 10, v225\n"
    "v_add_f32 v193, v193, v138\n"
    "v_mfma_f32_16x16x32_bf16 v[8:11], v[176:179], a[168:171], v[8:11]\n"
    "v_lshlrev_b32 v225, 4, v225\n"
    "v_add_f32 v193, v193, v139\n"
    "v_add_u32 v225, s79, v225\n"
    "v_mfma_f32_16x16x32_bf16 v[12:15], v[176:179], a[172:175], v[12:15]\n"
    "v_add_f32 v193, v193, v140\n"
    "v_and_b32 v227, 0xf0, v198\n"
    "v_add_f32 v193, v193, v141\n"
    "v_mfma_f32_16x16x32_bf16 v[16:19], v[176:179], a[176:179], v[16:19]\n"
    "v_add_f32 v193, v193, v142\n"
    "v_add_u32 v226, v226, v227\n"
    "v_add_f32 v193, v193, v143\n"
    "v_mfma_f32_16x16x32_bf16 v[20:23], v[176:179], a[180:183], v[20:23]\n"
    "s_movk_i32 s80, 0\n"
    "v_cvt_pk_bf16_f32 v180, v136, v137\n"
    "v_mfma_f32_16x16x32_bf16 v[24:27], v[176:179], a[184:187], v[24:27]\n"
    "s_movk_i32 s81, 4096\n"
    "v_cvt_pk_bf16_f32 v181, v138, v139\n"
    "s_movk_i32 s82, 8192\n"
    "v_mfma_f32_16x16x32_bf16 v[28:31], v[176:179], a[188:191], v[28:31]\n"
    "v_cvt_pk_bf16_f32 v182, v140, v141\n"
    "s_movk_i32 s83, 12288\n"
    "v_cvt_pk_bf16_f32 v183, v142, v143\n"
    "v_mfma_f32_16x16x32_bf16 v[152:155], a[64:67], a[48:51], v[172:175]\n"
    "v_mov_b32 v204, v192\n"
    "v_exp_f32 v144, v144\n"
    "v_mov_b32 v205, v204\n"
    "v_mfma_f32_16x16x32_bf16 v[152:155], a[68:71], a[52:55], v[152:155]\n"
    "v_exp_f32 v145, v145\n"
    "s_nop 1\n"
    "v_mfma_f32_16x16x32_bf16 v[152:155], a[72:75], a[56:59], v[152:155]\n"
    "v_exp_f32 v146, v146\n"
    "v_permlane16_swap_b32 v204, v205\n"
    "v_mfma_f32_16x16x32_bf16 v[152:155], a[76:79], a[60:63], v[152:155]\n"
    "v_add_f32 v204, v204, v205\n"
    "v_exp_f32 v147, v147\n"
    "v_mov_b32 v205, v204\n"
    "v_mfma_f32_16x16x32_bf16 v[156:159], a[80:83], a[48:51], v[172:175]\n"
    "v_exp_f32 v148, v148\n"
    "s_nop 1\n"
    "v_mfma_f32_16x16x32_bf16 v[156:159], a[84:87], a[52:55], v[156:159]\n"
    "v_exp_f32 v149, v149\n"
    "v_permlane32_swap_b32 v204, v205\n"
    "v_add_f32 v204, v204, v205\n"
    "v_mfma_f32_16x16x32_bf16 v[156:159], a[88:91], a[56:59], v[156:159]\n"
    "v_exp_f32 v150, v150\n"
    "v_rcp_f32 v204, v204\n"
    "v_mfma_f32_16x16x32_bf16 v[156:159], a[92:95], a[60:63], v[156:159]\n"
    "v_exp_f32 v151, v151\n"
    "s_nop 0\n"
    "v_add_f32 v194, v194, v144\n"
    "v_mfma_f32_16x16x32_bf16 v[32:35], v[180:183], a[160:163], v[32:35]\n"
    "ds_write_b32 v224, v204 offset:0\n"
    "v_mov_b32 v206, v193\n"
    "v_add_f32 v194, v194, v145\n"
    "v_mfma_f32_16x16x32_bf16 v[36:39], v[180:183], a[164:167], v[36:39]\n"
    "v_mov_b32 v207, v206\n"
    "v_add_f32 v194, v194, v146\n"
    "s_nop 1\n"
    "v_mfma_f32_16x16x32_bf16 v[40:43], v[180:183], a[168:171], v[40:43]\n"
    "v_add_f32 v194, v194, v147\n"
    "v_permlane16_swap_b32 v206, v207\n"
    "v_add_f32 v206, v206, v207\n"
    "v_add_f32 v194, v194, v148\n"
    "v_mfma_f32_16x16x32_bf16 v[44:47], v[180:183], a[172:175], v[44:47]\n"
    "v_mov_b32 v207, v206\n"
    "v_add_f32 v194, v194, v149\n"
    "s_nop 1\n"
    "v_mfma_f32_16x16x32_bf16 v[48:51], v[180:183], a[176:179], v[48:51]\n"
    "v_add_f32 v194, v194, v150\n"
    "v_permlane32_swap_b32 v206, v207\n"
    "v_add_f32 v206, v206, v207\n"
    "v_add_f32 v194, v194, v151\n"
    "v_mfma_f32_16x16x32_bf16 v[52:55], v[180:183], a[180:183], v[52:55]\n"
    "v_rcp_f32 v206, v206\n"
    "v_cvt_pk_bf16_f32 v184, v144, v145\n"
    "v_mfma_f32_16x16x32_bf16 v[56:59], v[180:183], a[184:187], v[56:59]\n"
    "s_nop 0\n"
    "v_cvt_pk_bf16_f32 v185, v146, v147\n"
    "ds_write_b32 v224, v206 offset:64\n"
    "s_waitcnt lgkmcnt(0)\n"
    "v_mfma_f32_16x16x32_bf16 v[60:63], v[180:183], a[188:191], v[60:63]\n"
    "v_cvt_pk_bf16_f32 v186, v148, v149\n"
    "ds_read_b128 v[160:163], v225 offset:0\n"
    "v_cvt_pk_bf16_f32 v187, v150, v151\n"
    "ds_read_b128 v[164:167], v225 offset:64\n"
    "v_mfma_f32_16x16x32_bf16 v[144:147], a[96:99], a[32:35], v[168:171]\n"
    "s_waitcnt lgkmcnt(0)\n"
    "v_exp_f32 v152, v152\n"
    "v_mul_f32 v0, v0, v160\n"
    "v_mul_f32 v1, v1, v161\n"
    "v_mfma_f32_16x16x32_bf16 v[144:147], a[100:103], a[36:39], v[144:147]\n"
    "v_exp_f32 v153, v153\n"
    "v_mul_f32 v4, v4, v160\n"
    "v_mul_f32 v5, v5, v161\n"
    "v_mul_f32 v8, v8, v160\n"
    "v_mfma_f32_16x16x32_bf16 v[144:147], a[104:107], a[40:43], v[144:147]\n"
    "v_exp_f32 v154, v154\n"
    "v_mul_f32 v9, v9, v161\n"
    "v_mul_f32 v12, v12, v160\n"
    "v_exp_f32 v155, v155\n"
    "v_mfma_f32_16x16x32_bf16 v[144:147], a[108:111], a[44:47], v[144:147]\n"
    "v_mul_f32 v13, v13, v161\n"
    "v_mul_f32 v16, v16, v160\n"
    "v_mul_f32 v17, v17, v161\n"
    "v_exp_f32 v156, v156\n"
    "v_mfma_f32_16x16x32_bf16 v[148:151], a[112:115], a[32:35], v[168:171]\n"
    "v_mul_f32 v20, v20, v160\n"
    "v_mul_f32 v21, v21, v161\n"
    "v_mul_f32 v24, v24, v160\n"
    "v_exp_f32 v157, v157\n"
    "v_mfma_f32_16x16x32_bf16 v[148:151], a[116:119], a[36:39], v[148:151]\n"
    "v_mul_f32 v25, v25, v161\n"
    "v_mul_f32 v28, v28, v160\n"
    "v_exp_f32 v158, v158\n"
    "v_mfma_f32_16x16x32_bf16 v[148:151], a[120:123], a[40:43], v[148:151]\n"
    "v_mul_f32 v29, v29, v161\n"
    "v_cvt_pk_bf16_f32 v228, v0, v4\n"
    "v_cvt_pk_bf16_f32 v229, v8, v12\n"
    "v_exp_f32 v159, v159\n"
    "v_mfma_f32_16x16x32_bf16 v[148:151], a[124:127], a[44:47], v[148:151]\n"
    "v_cvt_pk_bf16_f32 v230, v16, v20\n"
    "v_cvt_pk_bf16_f32 v231, v24, v28\n"
    "buffer_store_dwordx4 v[228:231], v226, s[44:47], s80 offen offset:0 sc1\n"
    "v_add_f32 v195, v195, v152\n"
    "v_cvt_pk_bf16_f32 v232, v1, v5\n"
    "v_cvt_pk_bf16_f32 v233, v9, v13\n"
    "v_mfma_f32_16x16x32_bf16 v[64:67], v[184:187], a[128:131], v[64:67]\n"
    "v_add_f32 v195, v195, v153\n"
    "v_cvt_pk_bf16_f32 v234, v17, v21\n"
    "v_cvt_pk_bf16_f32 v235, v25, v29\n"
    "buffer_store_dwordx4 v[232:235], v226, s[44:47], s80 offen offset:256 sc1\n"
    "v_add_f32 v195, v195, v154\n"
    "v_mfma_f32_16x16x32_bf16 v[68:71], v[184:187], a[132:135], v[68:71]\n"
    "v_mul_f32 v2, v2, v162\n"
    "v_mul_f32 v3, v3, v163\n"
    "v_mul_f32 v6, v6, v162\n"
    "v_add_f32 v195, v195, v155\n"
    "v_mul_f32 v7, v7, v163\n"
    "v_mfma_f32_16x16x32_bf16 v[72:75], v[184:187], a[136:139], v[72:75]\n"
    "v_mul_f32 v10, v10, v162\n"
    "v_add_f32 v195, v195, v156\n"
    "v_mul_f32 v11, v11, v163\n"
    "v_mul_f32 v14, v14, v162\n"
    "v_mul_f32 v15, v15, v163\n"
    "v_mfma_f32_16x16x32_bf16 v[76:79], v[184:187], a[140:143], v[76:79]\n"
    "v_add_f32 v195, v195, v157\n"
    "v_mul_f32 v18, v18, v162\n"
    "v_mul_f32 v19, v19, v163\n"
    "v_mul_f32 v22, v22, v162\n"
    "v_add_f32 v195, v195, v158\n"
    "v_mfma_f32_16x16x32_bf16 v[80:83], v[184:187], a[144:147], v[80:83]\n"
    "v_mul_f32 v23, v23, v163\n"
    "v_mul_f32 v26, v26, v162\n"
    "v_add_f32 v195, v195, v159\n"
    "v_mul_f32 v27, v27, v163\n"
    "v_mul_f32 v30, v30, v162\n"
    "v_mfma_f32_16x16x32_bf16 v[84:87], v[184:187], a[148:151], v[84:87]\n"
    "v_mul_f32 v31, v31, v163\n"
    "v_cvt_pk_bf16_f32 v188, v152, v153\n"
    "v_cvt_pk_bf16_f32 v236, v2, v6\n"
    "v_cvt_pk_bf16_f32 v237, v10, v14\n"
    "v_cvt_pk_bf16_f32 v238, v18, v22\n"
    "v_mfma_f32_16x16x32_bf16 v[88:91], v[184:187], a[152:155], v[88:91]\n"
    "v_cvt_pk_bf16_f32 v189, v154, v155\n"
    "v_cvt_pk_bf16_f32 v239, v26, v30\n"
    "buffer_store_dwordx4 v[236:239], v226, s[44:47], s80 offen offset:512 sc1\n"
    "v_cvt_pk_bf16_f32 v190, v156, v157\n"
    "v_cvt_pk_bf16_f32 v240, v3, v7\n"
    "v_mfma_f32_16x16x32_bf16 v[92:95], v[184:187], a[156:159], v[92:95]\n"
    "v_cvt_pk_bf16_f32 v241, v11, v15\n"
    "v_cvt_pk_bf16_f32 v242, v19, v23\n"
    "v_cvt_pk_bf16_f32 v191, v158, v159\n"
    "v_cvt_pk_bf16_f32 v243, v27, v31\n"
    "buffer_store_dwordx4 v[240:243], v226, s[44:47], s80 offen offset:768 sc1\n"
    "v_mfma_f32_16x16x32_bf16 v[152:155], a[96:99], a[48:51], v[172:175]\n"
    "v_mul_f32 v32, v32, v164\n"
    "v_exp_f32 v144, v144\n"
    "v_mfma_f32_16x16x32_bf16 v[152:155], a[100:103], a[52:55], v[152:155]\n"
    "v_mul_f32 v33, v33, v165\n"
    "v_exp_f32 v145, v145\n"
    "v_mul_f32 v36, v36, v164\n"
    "v_mfma_f32_16x16x32_bf16 v[152:155], a[104:107], a[56:59], v[152:155]\n"
    "v_exp_f32 v146, v146\n"
    "v_mul_f32 v37, v37, v165\n"
    "v_mfma_f32_16x16x32_bf16 v[152:155], a[108:111], a[60:63], v[152:155]\n"
    "v_exp_f32 v147, v147\n"
    "v_mul_f32 v40, v40, v164\n"
    "v_mul_f32 v41, v41, v165\n"
    "v_mfma_f32_16x16x32_bf16 v[156:159], a[112:115], a[48:51], v[172:175]\n"
    "v_exp_f32 v148, v148\n"
    "v_mul_f32 v44, v44, v164\n"
    "v_mfma_f32_16x16x32_bf16 v[156:159], a[116:119], a[52:55], v[156:159]\n"
    "v_exp_f32 v149, v149\n"
    "v_mul_f32 v45, v45, v165\n"
    "v_mfma_f32_16x16x32_bf16 v[156:159], a[120:123], a[56:59], v[156:159]\n"
    "v_exp_f32 v150, v150\n"
    "v_mul_f32 v48, v48, v164\n"
    "v_mul_f32 v49, v49, v165\n"
    "v_mfma_f32_16x16x32_bf16 v[156:159], a[124:127], a[60:63], v[156:159]\n"
    "v_exp_f32 v151, v151\n"
    "v_mul_f32 v52, v52, v164\n"
    "v_mfma_f32_16x16x32_bf16 v[96:99], v[188:191], a[128:131], v[96:99]\n"
    "v_add_f32 v194, v194, v144\n"
    "v_mul_f32 v53, v53, v165\n"
    "v_add_f32 v194, v194, v145\n"
    "v_mfma_f32_16x16x32_bf16 v[100:103], v[188:191], a[132:135], v[100:103]\n"
    "v_mul_f32 v56, v56, v164\n"
    "v_mul_f32 v57, v57, v165\n"
    "v_add_f32 v194, v194, v146\n"
    "v_mul_f32 v60, v60, v164\n"
    "v_mfma_f32_16x16x32_bf16 v[104:107], v[188:191], a[136:139], v[104:107]\n"
    "v_add_f32 v194, v194, v147\n"
    "v_mul_f32 v61, v61, v165\n"
    "v_add_f32 v194, v194, v148\n"
    "v_mfma_f32_16x16x32_bf16 v[108:111], v[188:191], a[140:143], v[108:111]\n"
    "v_cvt_pk_bf16_f32 v228, v32, v36\n"
    "v_add_f32 v194, v194, v149\n"
    "v_cvt_pk_bf16_f32 v229, v40, v44\n"
    "v_cvt_pk_bf16_f32 v230, v48, v52\n"
    "v_mfma_f32_16x16x32_bf16 v[112:115], v[188:191], a[144:147], v[112:115]\n"
    "v_add_f32 v194, v194, v150\n"
    "v_cvt_pk_bf16_f32 v231, v56, v60\n"
    "v_add_f32 v194, v194, v151\n"
    "v_mfma_f32_16x16x32_bf16 v[116:119], v[188:191], a[148:151], v[116:119]\n"
    "buffer_store_dwordx4 v[228:231], v226, s[44:47], s81 offen offset:0 sc1\n"
    "v_cvt_pk_bf16_f32 v184, v144, v145\n"
    "v_cvt_pk_bf16_f32 v232, v33, v37\n"
    "v_mfma_f32_16x16x32_bf16 v[120:123], v[188:191], a[152:155], v[120:123]\n"
    "v_cvt_pk_bf16_f32 v233, v41, v45\n"
    "v_cvt_pk_bf16_f32 v185, v146, v147\n"
    "v_cvt_pk_bf16_f32 v234, v49, v53\n"
    "v_cvt_pk_bf16_f32 v186, v148, v149\n"
    "v_mfma_f32_16x16x32_bf16 v[124:127], v[188:191], a[156:159], v[124:127]\n"
    "v_cvt_pk_bf16_f32 v235, v57, v61\n"
    "v_cvt_pk_bf16_f32 v187, v150, v151\n"
    "buffer_store_dwordx4 v[232:235], v226, s[44:47], s81 offen offset:256 sc1\n"
    "s_nop 1\n"
    "v_mfma_f32_16x16x32_bf16 v[64:67], v[184:187], a[160:163], v[64:67]\n"
    "v_mul_f32 v34, v34, v166\n"
    "v_mul_f32 v35, v35, v167\n"
    "v_mul_f32 v38, v38, v166\n"
    "v_mul_f32 v39, v39, v167\n"
    "v_mul_f32 v42, v42, v166\n"
    "v_mul_f32 v43, v43, v167\n"
    "v_mfma_f32_16x16x32_bf16 v[68:71], v[184:187], a[164:167], v[68:71]\n"
    "v_exp_f32 v152, v152\n"
    "v_exp_f32 v153, v153\n"
    "v_exp_f32 v154, v154\n"
    "v_exp_f32 v155, v155\n"
    "v_mfma_f32_16x16x32_bf16 v[72:75], v[184:187], a[168:171], v[72:75]\n"
    "v_exp_f32 v156, v156\n"
    "v_exp_f32 v157, v157\n"
    "v_exp_f32 v158, v158\n"
    "v_mfma_f32_16x16x32_bf16 v[76:79], v[184:187], a[172:175], v[76:79]\n"
    "v_exp_f32 v159, v159\n"
    "v_add_f32 v195, v195, v152\n"
    "v_add_f32 v195, v195, v153\n"
    "v_add_f32 v195, v195, v154\n"
    "v_add_f32 v195, v195, v155\n"
    "v_add_f32 v195, v195, v156\n"
    "v_mfma_f32_16x16x32_bf16 v[80:83], v[184:187], a[176:179], v[80:83]\n"
    "v_add_f32 v195, v195, v157\n"
    "v_add_f32 v195, v195, v158\n"
    "v_add_f32 v195, v195, v159\n"
    "v_cvt_pk_bf16_f32 v188, v152, v153\n"
    "v_cvt_pk_bf16_f32 v189, v154, v155\n"
    "v_cvt_pk_bf16_f32 v190, v156, v157\n"
    "v_cvt_pk_bf16_f32 v191, v158, v159\n"
    "v_mfma_f32_16x16x32_bf16 v[84:87], v[184:187], a[180:183], v[84:87]\n"
    "v_mul_f32 v46, v46, v166\n"
    "v_mul_f32 v47, v47, v167\n"
    "v_mul_f32 v50, v50, v166\n"
    "v_mul_f32 v51, v51, v167\n"
    "v_mul_f32 v54, v54, v166\n"
    "v_mul_f32 v55, v55, v167\n"
    "v_mul_f32 v58, v58, v166\n"
    "v_mfma_f32_16x16x32_bf16 v[88:91], v[184:187], a[184:187], v[88:91]\n"
    "v_mul_f32 v59, v59, v167\n"
    "v_mul_f32 v62, v62, v166\n"
    "v_mul_f32 v63, v63, v167\n"
    "v_cvt_pk_bf16_f32 v236, v34, v38\n"
    "v_cvt_pk_bf16_f32 v237, v42, v46\n"
    "v_cvt_pk_bf16_f32 v238, v50, v54\n"
    "v_mfma_f32_16x16x32_bf16 v[92:95], v[184:187], a[188:191], v[92:95]\n"
    "v_cvt_pk_bf16_f32 v239, v58, v62\n"
    "buffer_store_dwordx4 v[236:239], v226, s[44:47], s81 offen offset:512 sc1\n"
    "v_cvt_pk_bf16_f32 v240, v35, v39\n"
    "v_cvt_pk_bf16_f32 v241, v43, v47\n"
    "v_cvt_pk_bf16_f32 v242, v51, v55\n"
    "v_cvt_pk_bf16_f32 v243, v59, v63\n"
    "buffer_store_dwordx4 v[240:243], v226, s[44:47], s81 offen offset:768 sc1\n"
    "s_nop 1\n"
    "v_mfma_f32_16x16x32_bf16 v[96:99], v[188:191], a[160:163], v[96:99]\n"
    "v_add_f32 v214, v192, v193\n"
    "v_add_f32 v215, v194, v195\n"
    "v_add_f32 v214, v214, v215\n"
    "v_cmp_nge_f32 vcc, s55, v214\n"
    "s_cbranch_vccnz .Lfallback\n"
    "v_mfma_f32_16x16x32_bf16 v[100:103], v[188:191], a[164:167], v[100:103]\n"
    "v_mov_b32 v208, v194\n"
    "v_mov_b32 v209, v208\n"
    "s_nop 1\n"
    "v_permlane16_swap_b32 v208, v209\n"
    "v_mfma_f32_16x16x32_bf16 v[104:107], v[188:191], a[168:171], v[104:107]\n"
    "v_add_f32 v208, v208, v209\n"
    "v_mov_b32 v209, v208\n"
    "s_nop 1\n"
    "v_mfma_f32_16x16x32_bf16 v[108:111], v[188:191], a[172:175], v[108:111]\n"
    "v_permlane32_swap_b32 v208, v209\n"
    "v_add_f32 v208, v208, v209\n"
    "v_rcp_f32 v208, v208\n"
    "s_nop 0\n"
    "v_mfma_f32_16x16x32_bf16 v[112:115], v[188:191], a[176:179], v[112:115]\n"
    "ds_write_b32 v224, v208 offset:128\n"
    "v_mov_b32 v210, v195\n"
    "v_mov_b32 v211, v210\n"
    "s_nop 1\n"
    "v_mfma_f32_16x16x32_bf16 v[116:119], v[188:191], a[180:183], v[116:119]\n"
    "v_permlane16_swap_b32 v210, v211\n"
    "v_add_f32 v210, v210, v211\n"
    "v_mov_b32 v211, v210\n"
    "s_nop 1\n"
    "v_mfma_f32_16x16x32_bf16 v[120:123], v[188:191], a[184:187], v[120:123]\n"
    "v_permlane32_swap_b32 v210, v211\n"
    "v_add_f32 v210, v210, v211\n"
    "v_rcp_f32 v210, v210\n"
    "v_mfma_f32_16x16x32_bf16 v[124:127], v[188:191], a[188:191], v[124:127]\n"
    "s_nop 0\n"
    "ds_write_b32 v224, v210 offset:192\n"
    "s_waitcnt lgkmcnt(0)\n"
    "ds_read_b128 v[168:171], v225 offset:128\n"
    "ds_read_b128 v[172:175], v225 offset:192\n"
    "s_waitcnt lgkmcnt(0)\n"
    "v_pk_mul_f32 v[64:65], v[64:65], v[168:169]\n"
    "v_pk_mul_f32 v[68:69], v[68:69], v[168:169]\n"
    "v_pk_mul_f32 v[72:73], v[72:73], v[168:169]\n"
    "v_pk_mul_f32 v[76:77], v[76:77], v[168:169]\n"
    "v_pk_mul_f32 v[80:81], v[80:81], v[168:169]\n"
    "v_pk_mul_f32 v[84:85], v[84:85], v[168:169]\n"
    "v_pk_mul_f32 v[88:89], v[88:89], v[168:169]\n"
    "v_pk_mul_f32 v[92:93], v[92:93], v[168:169]\n"
    "v_cvt_pk_bf16_f32 v228, v64, v68\n"
    "v_cvt_pk_bf16_f32 v229, v72, v76\n"
    "v_cvt_pk_bf16_f32 v230, v80, v84\n"
    "v_cvt_pk_bf16_f32 v231, v88, v92\n"
    "buffer_store_dwordx4 v[228:231], v226, s[44:47], s82 offen offset:0 sc1\n"
    "v_cvt_pk_bf16_f32 v232, v65, v69\n"
    "v_cvt_pk_bf16_f32 v233, v73, v77\n"
    "v_cvt_pk_bf16_f32 v234, v81, v85\n"
    "v_cvt_pk_bf16_f32 v235, v89, v93\n"
    "buffer_store_dwordx4 v[232:235], v226, s[44:47], s82 offen offset:256 sc1\n"
    "v_pk_mul_f32 v[66:67], v[66:67], v[170:171]\n"
    "v_pk_mul_f32 v[70:71], v[70:71], v[170:171]\n"
    "v_pk_mul_f32 v[74:75], v[74:75], v[170:171]\n"
    "v_pk_mul_f32 v[78:79], v[78:79], v[170:171]\n"
    "v_pk_mul_f32 v[82:83], v[82:83], v[170:171]\n"
    "v_pk_mul_f32 v[86:87], v[86:87], v[170:171]\n"
    "v_pk_mul_f32 v[90:91], v[90:91], v[170:171]\n"
    "v_pk_mul_f32 v[94:95], v[94:95], v[170:171]\n"
    "v_cvt_pk_bf16_f32 v236, v66, v70\n"
    "v_cvt_pk_bf16_f32 v237, v74, v78\n"
    "v_cvt_pk_bf16_f32 v238, v82, v86\n"
    "v_cvt_pk_bf16_f32 v239, v90, v94\n"
    "buffer_store_dwordx4 v[236:239], v226, s[44:47], s82 offen offset:512 sc1\n"
    "v_cvt_pk_bf16_f32 v240, v67, v71\n"
    "v_cvt_pk_bf16_f32 v241, v75, v79\n"
    "v_cvt_pk_bf16_f32 v242, v83, v87\n"
    "v_cvt_pk_bf16_f32 v243, v91, v95\n"
    "buffer_store_dwordx4 v[240:243], v226, s[44:47], s82 offen offset:768 sc1\n"
    "v_pk_mul_f32 v[96:97], v[96:97], v[172:173]\n"
    "v_pk_mul_f32 v[100:101], v[100:101], v[172:173]\n"
    "v_pk_mul_f32 v[104:105], v[104:105], v[172:173]\n"
    "v_pk_mul_f32 v[108:109], v[108:109], v[172:173]\n"
    "v_pk_mul_f32 v[112:113], v[112:113], v[172:173]\n"
    "v_pk_mul_f32 v[116:117], v[116:117], v[172:173]\n"
    "v_pk_mul_f32 v[120:121], v[120:121], v[172:173]\n"
    "v_pk_mul_f32 v[124:125], v[124:125], v[172:173]\n"
    "v_cvt_pk_bf16_f32 v228, v96, v100\n"
    "v_cvt_pk_bf16_f32 v229, v104, v108\n"
    "v_cvt_pk_bf16_f32 v230, v112, v116\n"
    "v_cvt_pk_bf16_f32 v231, v120, v124\n"
    "buffer_store_dwordx4 v[228:231], v226, s[44:47], s83 offen offset:0 sc1\n"
    "v_cvt_pk_bf16_f32 v232, v97, v101\n"
    "v_cvt_pk_bf16_f32 v233, v105, v109\n"
    "v_cvt_pk_bf16_f32 v234, v113, v117\n"
    "v_cvt_pk_bf16_f32 v235, v121, v125\n"
    "buffer_store_dwordx4 v[232:235], v226, s[44:47], s83 offen offset:256 sc1\n"
    "v_pk_mul_f32 v[98:99], v[98:99], v[174:175]\n"
    "v_pk_mul_f32 v[102:103], v[102:103], v[174:175]\n"
    "v_pk_mul_f32 v[106:107], v[106:107], v[174:175]\n"
    "v_pk_mul_f32 v[110:111], v[110:111], v[174:175]\n"
    "v_pk_mul_f32 v[114:115], v[114:115], v[174:175]\n"
    "v_pk_mul_f32 v[118:119], v[118:119], v[174:175]\n"
    "v_pk_mul_f32 v[122:123], v[122:123], v[174:175]\n"
    "v_pk_mul_f32 v[126:127], v[126:127], v[174:175]\n"
    "v_cvt_pk_bf16_f32 v236, v98, v102\n"
    "v_cvt_pk_bf16_f32 v237, v106, v110\n"
    "v_cvt_pk_bf16_f32 v238, v114, v118\n"
    "v_cvt_pk_bf16_f32 v239, v122, v126\n"
    "buffer_store_dwordx4 v[236:239], v226, s[44:47], s83 offen offset:512 sc1\n"
    "v_cvt_pk_bf16_f32 v240, v99, v103\n"
    "v_cvt_pk_bf16_f32 v241, v107, v111\n"
    "v_cvt_pk_bf16_f32 v242, v115, v119\n"
    "v_cvt_pk_bf16_f32 v243, v123, v127\n"
    "buffer_store_dwordx4 v[240:243], v226, s[44:47], s83 offen offset:768 sc1\n"
    ".Lfin:\n"
    "s_lshl_b32 s56, s59, 7\n"
    "v_mov_b32 v253, s56\n"
    "v_mov_b32 v254, 1\n"
    "v_mov_b32 v255, 0\n"
    "s_mov_b64 exec, 1\n"
    "global_atomic_add_x2 v253, v[254:255], %[ctl] offset:1024\n"
    "s_mov_b64 exec, -1\n"
    "s_branch .Lend\n"
    ".Lslow0:\n"
    "ds_read_b32 v252, v200\n"
    "s_waitcnt lgkmcnt(0)\n"
    "v_cmp_gt_u32 vcc, s87, v252\n"
    "s_cbranch_vccnz .Lslow0\n"
    "s_branch .Lback0\n"
    ".Lslow1:\n"
    "ds_read_b32 v252, v200\n"
    "s_waitcnt lgkmcnt(0)\n"
    "v_cmp_gt_u32 vcc, s87, v252\n"
    "s_cbranch_vccnz .Lslow1\n"
    "s_branch .Lback1\n"
    ".Lfallback:\n"
    "s_nop 7\n"
    "s_nop 7\n"
    "s_waitcnt vmcnt(0) lgkmcnt(0)\n"
    "v_mov_b32 v0, 0\n"
    "v_mov_b32 v1, 0\n"
    "v_mov_b32 v2, 0\n"
    "v_mov_b32 v3, 0\n"
    "v_mov_b32 v4, 0\n"
    "v_mov_b32 v5, 0\n"
    "v_mov_b32 v6, 0\n"
    "v_mov_b32 v7, 0\n"
    "v_mov_b32 v8, 0\n"
    "v_mov_b32 v9, 0\n"
    "v_mov_b32 v10, 0\n"
    "v_mov_b32 v11, 0\n"
    "v_mov_b32 v12, 0\n"
    "v_mov_b32 v13, 0\n"
    "v_mov_b32 v14, 0\n"
    "v_mov_b32 v15, 0\n"
    "v_mov_b32 v16, 0\n"
    "v_mov_b32 v17, 0\n"
    "v_mov_b32 v18, 0\n"
    "v_mov_b32 v19, 0\n"
    "v_mov_b32 v20, 0\n"
    "v_mov_b32 v21, 0\n"
    "v_mov_b32 v22, 0\n"
    "v_mov_b32 v23, 0\n"
    "v_mov_b32 v24, 0\n"
    "v_mov_b32 v25, 0\n"
    "v_mov_b32 v26, 0\n"
    "v_mov_b32 v27, 0\n"
    "v_mov_b32 v28, 0\n"
    "v_mov_b32 v29, 0\n"
    "v_mov_b32 v30, 0\n"
    "v_mov_b32 v31, 0\n"
    "v_mov_b32 v32, 0\n"
    "v_mov_b32 v33, 0\n"
    "v_mov_b32 v34, 0\n"
    "v_mov_b32 v35, 0\n"
    "v_mov_b32 v36, 0\n"
    "v_mov_b32 v37, 0\n"
    "v_mov_b32 v38, 0\n"
    "v_mov_b32 v39, 0\n"
    "v_mov_b32 v40, 0\n"
    "v_mov_b32 v41, 0\n"
    "v_mov_b32 v42, 0\n"
    "v_mov_b32 v43, 0\n"
    "v_mov_b32 v44, 0\n"
    "v_mov_b32 v45, 0\n"
    "v_mov_b32 v46, 0\n"
    "v_mov_b32 v47, 0\n"
    "v_mov_b32 v48, 0\n"
    "v_mov_b32 v49, 0\n"
    "v_mov_b32 v50, 0\n"
    "v_mov_b32 v51, 0\n"
    "v_mov_b32 v52, 0\n"
    "v_mov_b32 v53, 0\n"
    "v_mov_b32 v54, 0\n"
    "v_mov_b32 v55, 0\n"
    "v_mov_b32 v56, 0\n"
    "v_mov_b32 v57, 0\n"
    "v_mov_b32 v58, 0\n"
    "v_mov_b32 v59, 0\n"
    "v_mov_b32 v60, 0\n"
    "v_mov_b32 v61, 0\n"
    "v_mov_b32 v62, 0\n"
    "v_mov_b32 v63, 0\n"
    "v_mov_b32 v64, 0\n"
    "v_mov_b32 v65, 0\n"
    "v_mov_b32 v66, 0\n"
    "v_mov_b32 v67, 0\n"
    "v_mov_b32 v68, 0\n"
    "v_mov_b32 v69, 0\n"
    "v_mov_b32 v70, 0\n"
    "v_mov_b32 v71, 0\n"
    "v_mov_b32 v72, 0\n"
    "v_mov_b32 v73, 0\n"
    "v_mov_b32 v74, 0\n"
    "v_mov_b32 v75, 0\n"
    "v_mov_b32 v76, 0\n"
    "v_mov_b32 v77, 0\n"
    "v_mov_b32 v78, 0\n"
    "v_mov_b32 v79, 0\n"
    "v_mov_b32 v80, 0\n"
    "v_mov_b32 v81, 0\n"
    "v_mov_b32 v82, 0\n"
    "v_mov_b32 v83, 0\n"
    "v_mov_b32 v84, 0\n"
    "v_mov_b32 v85, 0\n"
    "v_mov_b32 v86, 0\n"
    "v_mov_b32 v87, 0\n"
    "v_mov_b32 v88, 0\n"
    "v_mov_b32 v89, 0\n"
    "v_mov_b32 v90, 0\n"
    "v_mov_b32 v91, 0\n"
    "v_mov_b32 v92, 0\n"
    "v_mov_b32 v93, 0\n"
    "v_mov_b32 v94, 0\n"
    "v_mov_b32 v95, 0\n"
    "v_mov_b32 v96, 0\n"
    "v_mov_b32 v97, 0\n"
    "v_mov_b32 v98, 0\n"
    "v_mov_b32 v99, 0\n"
    "v_mov_b32 v100, 0\n"
    "v_mov_b32 v101, 0\n"
    "v_mov_b32 v102, 0\n"
    "v_mov_b32 v103, 0\n"
    "v_mov_b32 v104, 0\n"
    "v_mov_b32 v105, 0\n"
    "v_mov_b32 v106, 0\n"
    "v_mov_b32 v107, 0\n"
    "v_mov_b32 v108, 0\n"
    "v_mov_b32 v109, 0\n"
    "v_mov_b32 v110, 0\n"
    "v_mov_b32 v111, 0\n"
    "v_mov_b32 v112, 0\n"
    "v_mov_b32 v113, 0\n"
    "v_mov_b32 v114, 0\n"
    "v_mov_b32 v115, 0\n"
    "v_mov_b32 v116, 0\n"
    "v_mov_b32 v117, 0\n"
    "v_mov_b32 v118, 0\n"
    "v_mov_b32 v119, 0\n"
    "v_mov_b32 v120, 0\n"
    "v_mov_b32 v121, 0\n"
    "v_mov_b32 v122, 0\n"
    "v_mov_b32 v123, 0\n"
    "v_mov_b32 v124, 0\n"
    "v_mov_b32 v125, 0\n"
    "v_mov_b32 v126, 0\n"
    "v_mov_b32 v127, 0\n"
    "v_mov_b32 v192, 0\n"
    "v_mov_b32 v248, 0xff7fffff\n"
    "v_mov_b32 v193, 0\n"
    "v_mov_b32 v249, 0xff7fffff\n"
    "v_mov_b32 v194, 0\n"
    "v_mov_b32 v250, 0xff7fffff\n"
    "v_mov_b32 v195, 0\n"
    "v_mov_b32 v251, 0xff7fffff\n"
    "s_mov_b32 s57, 0\n"
    ".Lfb_loop:\n"
    "s_add_u32 s58, s57, 0\n"
    "buffer_load_dwordx4 a[64:67], v198, s[40:43], s58 offen offset:0 sc1\n"
    "buffer_load_dwordx4 a[68:71], v198, s[40:43], s58 offen offset:1024 sc1\n"
    "buffer_load_dwordx4 a[72:75], v198, s[40:43], s58 offen offset:2048 sc1\n"
    "buffer_load_dwordx4 a[76:79], v198, s[40:43], s58 offen offset:3072 sc1\n"
    "s_add_u32 s58, s57, 4096\n"
    "buffer_load_dwordx4 a[80:83], v198, s[40:43], s58 offen offset:0 sc1\n"
    "buffer_load_dwordx4 a[84:87], v198, s[40:43], s58 offen offset:1024 sc1\n"
    "buffer_load_dwordx4 a[88:91], v198, s[40:43], s58 offen offset:2048 sc1\n"
    "buffer_load_dwordx4 a[92:95], v198, s[40:43], s58 offen offset:3072 sc1\n"
    "s_add_u32 s58, s57, 8192\n"
    "buffer_load_dwordx4 a[128:131], v198, s[40:43], s58 offen offset:0 sc1\n"
    "buffer_load_dwordx4 a[132:135], v198, s[40:43], s58 offen offset:1024 sc1\n"
    "buffer_load_dwordx4 a[136:139], v198, s[40:43], s58 offen offset:2048 sc1\n"
    "buffer_load_dwordx4 a[140:143], v198, s[40:43], s58 offen offset:3072 sc1\n"
    "s_add_u32 s58, s57, 12288\n"
    "buffer_load_dwordx4 a[144:147], v198, s[40:43], s58 offen offset:0 sc1\n"
    "buffer_load_dwordx4 a[148:151], v198, s[40:43], s58 offen offset:1024 sc1\n"
    "buffer_load_dwordx4 a[152:155], v198, s[40:43], s58 offen offset:2048 sc1\n"
    "buffer_load_dwordx4 a[156:159], v198, s[40:43], s58 offen offset:3072 sc1\n"
    "s_waitcnt vmcnt(0)\n"
    "v_mfma_f32_16x16x32_bf16 v[128:131], a[64:67], a[0:3], 0\n"
    "v_mfma_f32_16x16x32_bf16 v[128:131], a[68:71], a[4:7], v[128:131]\n"
    "v_mfma_f32_16x16x32_bf16 v[128:131], a[72:75], a[8:11], v[128:131]\n"
    "v_mfma_f32_16x16x32_bf16 v[128:131], a[76:79], a[12:15], v[128:131]\n"
    "v_mfma_f32_16x16x32_bf16 v[132:135], a[80:83], a[0:3], 0\n"
    "v_mfma_f32_16x16x32_bf16 v[132:135], a[84:87], a[4:7], v[132:135]\n"
    "v_mfma_f32_16x16x32_bf16 v[132:135], a[88:91], a[8:11], v[132:135]\n"
    "v_mfma_f32_16x16x32_bf16 v[132:135], a[92:95], a[12:15], v[132:135]\n"
    "s_nop 7\n"
    "v_max3_f32 v204, v128, v129, v130\n"
    "v_max3_f32 v204, v204, v131, v132\n"
    "v_max3_f32 v204, v204, v133, v134\n"
    "v_max_f32 v204, v204, v135\n"
    "v_mov_b32 v205, v204\n"
    "s_nop 1\n"
    "v_permlane16_swap_b32 v204, v205\n"
    "v_max_f32 v204, v204, v205\n"
    "v_mov_b32 v205, v204\n"
    "s_nop 1\n"
    "v_permlane32_swap_b32 v204, v205\n"
    "v_max_f32 v204, v204, v205\n"
    "v_max_f32 v204, v204, v248\n"
    "v_sub_f32 v206, v248, v204\n"
    "v_exp_f32 v206, v206\n"
    "v_mov_b32 v248, v204\n"
    "v_mul_f32 v192, v192, v206\n"
    "ds_write_b32 v224, v206 offset:0\n"
    "v_sub_f32 v128, v128, v204\n"
    "v_sub_f32 v129, v129, v204\n"
    "v_sub_f32 v130, v130, v204\n"
    "v_sub_f32 v131, v131, v204\n"
    "v_sub_f32 v132, v132, v204\n"
    "v_sub_f32 v133, v133, v204\n"
    "v_sub_f32 v134, v134, v204\n"
    "v_sub_f32 v135, v135, v204\n"
    "v_exp_f32 v128, v128\n"
    "v_exp_f32 v129, v129\n"
    "v_exp_f32 v130, v130\n"
    "v_exp_f32 v131, v131\n"
    "v_exp_f32 v132, v132\n"
    "v_exp_f32 v133, v133\n"
    "v_exp_f32 v134, v134\n"
    "v_exp_f32 v135, v135\n"
    "v_add_f32 v192, v192, v128\n"
    "v_add_f32 v192, v192, v129\n"
    "v_add_f32 v192, v192, v130\n"
    "v_add_f32 v192, v192, v131\n"
    "v_add_f32 v192, v192, v132\n"
    "v_add_f32 v192, v192, v133\n"
    "v_add_f32 v192, v192, v134\n"
    "v_add_f32 v192, v192, v135\n"
    "v_cvt_pk_bf16_f32 v176, v128, v129\n"
    "v_cvt_pk_bf16_f32 v177, v130, v131\n"
    "v_cvt_pk_bf16_f32 v178, v132, v133\n"
    "v_cvt_pk_bf16_f32 v179, v134, v135\n"
    "s_waitcnt lgkmcnt(0)\n"
    "ds_read_b128 v[244:247], v225 offset:0\n"
    "s_waitcnt lgkmcnt(0)\n"
    "v_mul_f32 v0, v0, v244\n"
    "v_mul_f32 v1, v1, v245\n"
    "v_mul_f32 v2, v2, v246\n"
    "v_mul_f32 v3, v3, v247\n"
    "v_mul_f32 v4, v4, v244\n"
    "v_mul_f32 v5, v5, v245\n"
    "v_mul_f32 v6, v6, v246\n"
    "v_mul_f32 v7, v7, v247\n"
    "v_mul_f32 v8, v8, v244\n"
    "v_mul_f32 v9, v9, v245\n"
    "v_mul_f32 v10, v10, v246\n"
    "v_mul_f32 v11, v11, v247\n"
    "v_mul_f32 v12, v12, v244\n"
    "v_mul_f32 v13, v13, v245\n"
    "v_mul_f32 v14, v14, v246\n"
    "v_mul_f32 v15, v15, v247\n"
    "v_mul_f32 v16, v16, v244\n"
    "v_mul_f32 v17, v17, v245\n"
    "v_mul_f32 v18, v18, v246\n"
    "v_mul_f32 v19, v19, v247\n"
    "v_mul_f32 v20, v20, v244\n"
    "v_mul_f32 v21, v21, v245\n"
    "v_mul_f32 v22, v22, v246\n"
    "v_mul_f32 v23, v23, v247\n"
    "v_mul_f32 v24, v24, v244\n"
    "v_mul_f32 v25, v25, v245\n"
    "v_mul_f32 v26, v26, v246\n"
    "v_mul_f32 v27, v27, v247\n"
    "v_mul_f32 v28, v28, v244\n"
    "v_mul_f32 v29, v29, v245\n"
    "v_mul_f32 v30, v30, v246\n"
    "v_mul_f32 v31, v31, v247\n"
    "s_nop 1\n"
    "v_mfma_f32_16x16x32_bf16 v[0:3], v[176:179], a[128:131], v[0:3]\n"
    "v_mfma_f32_16x16x32_bf16 v[4:7], v[176:179], a[132:135], v[4:7]\n"
    "v_mfma_f32_16x16x32_bf16 v[8:11], v[176:179], a[136:139], v[8:11]\n"
    "v_mfma_f32_16x16x32_bf16 v[12:15], v[176:179], a[140:143], v[12:15]\n"
    "v_mfma_f32_16x16x32_bf16 v[16:19], v[176:179], a[144:147], v[16:19]\n"
    "v_mfma_f32_16x16x32_bf16 v[20:23], v[176:179], a[148:151], v[20:23]\n"
    "v_mfma_f32_16x16x32_bf16 v[24:27], v[176:179], a[152:155], v[24:27]\n"
    "v_mfma_f32_16x16x32_bf16 v[28:31], v[176:179], a[156:159], v[28:31]\n"
    "v_mfma_f32_16x16x32_bf16 v[136:139], a[64:67], a[16:19], 0\n"
    "v_mfma_f32_16x16x32_bf16 v[136:139], a[68:71], a[20:23], v[136:139]\n"
    "v_mfma_f32_16x16x32_bf16 v[136:139], a[72:75], a[24:27], v[136:139]\n"
    "v_mfma_f32_16x16x32_bf16 v[136:139], a[76:79], a[28:31], v[136:139]\n"
    "v_mfma_f32_16x16x32_bf16 v[140:143], a[80:83], a[16:19], 0\n"
    "v_mfma_f32_16x16x32_bf16 v[140:143], a[84:87], a[20:23], v[140:143]\n"
    "v_mfma_f32_16x16x32_bf16 v[140:143], a[88:91], a[24:27], v[140:143]\n"
    "v_mfma_f32_16x16x32_bf16 v[140:143], a[92:95], a[28:31], v[140:143]\n"
    "s_nop 7\n"
    "v_max3_f32 v204, v136, v137, v138\n"
    "v_max3_f32 v204, v204, v139, v140\n"
    "v_max3_f32 v204, v204, v141, v142\n"
    "v_max_f32 v204, v204, v143\n"
    "v_mov_b32 v205, v204\n"
    "s_nop 1\n"
    "v_permlane16_swap_b32 v204, v205\n"
    "v_max_f32 v204, v204, v205\n"
    "v_mov_b32 v205, v204\n"
    "s_nop 1\n"
    "v_permlane32_swap_b32 v204, v205\n"
    "v_max_f32 v204, v204, v205\n"
    "v_max_f32 v204, v204, v249\n"
    "v_sub_f32 v206, v249, v204\n"
    "v_exp_f32 v206, v206\n"
    "v_mov_b32 v249, v204\n"
    "v_mul_f32 v193, v193, v206\n"
    "ds_write_b32 v224, v206 offset:0\n"
    "v_sub_f32 v136, v136, v204\n"
    "v_sub_f32 v137, v137, v204\n"
    "v_sub_f32 v138, v138, v204\n"
    "v_sub_f32 v139, v139, v204\n"
    "v_sub_f32 v140, v140, v204\n"
    "v_sub_f32 v141, v141, v204\n"
    "v_sub_f32 v142, v142, v204\n"
    "v_sub_f32 v143, v143, v204\n"
    "v_exp_f32 v136, v136\n"
    "v_exp_f32 v137, v137\n"
    "v_exp_f32 v138, v138\n"
    "v_exp_f32 v139, v139\n"
    "v_exp_f32 v140, v140\n"
    "v_exp_f32 v141, v141\n"
    "v_exp_f32 v142, v142\n"
    "v_exp_f32 v143, v143\n"
    "v_add_f32 v193, v193, v136\n"
    "v_add_f32 v193, v193, v137\n"
    "v_add_f32 v193, v193, v138\n"
    "v_add_f32 v193, v193, v139\n"
    "v_add_f32 v193, v193, v140\n"
    "v_add_f32 v193, v193, v141\n"
    "v_add_f32 v193, v193, v142\n"
    "v_add_f32 v193, v193, v143\n"
    "v_cvt_pk_bf16_f32 v180, v136, v137\n"
    "v_cvt_pk_bf16_f32 v181, v138, v139\n"
    "v_cvt_pk_bf16_f32 v182, v140, v141\n"
    "v_cvt_pk_bf16_f32 v183, v142, v143\n"
    "s_waitcnt lgkmcnt(0)\n"
    "ds_read_b128 v[244:247], v225 offset:0\n"
    "s_waitcnt lgkmcnt(0)\n"
    "v_mul_f32 v32, v32, v244\n"
    "v_mul_f32 v33, v33, v245\n"
    "v_mul_f32 v34, v34, v246\n"
    "v_mul_f32 v35, v35, v247\n"
    "v_mul_f32 v36, v36, v244\n"
    "v_mul_f32 v37, v37, v245\n"
    "v_mul_f32 v38, v38, v246\n"
    "v_mul_f32 v39, v39, v247\n"
    "v_mul_f32 v40, v40, v244\n"
    "v_mul_f32 v41, v41, v245\n"
    "v_mul_f32 v42, v42, v246\n"
    "v_mul_f32 v43, v43, v247\n"
    "v_mul_f32 v44, v44, v244\n"
    "v_mul_f32 v45, v45, v245\n"
    "v_mul_f32 v46, v46, v246\n"
    "v_mul_f32 v47, v47, v247\n"
    "v_mul_f32 v48, v48, v244\n"
    "v_mul_f32 v49, v49, v245\n"
    "v_mul_f32 v50, v50, v246\n"
    "v_mul_f32 v51, v51, v247\n"
    "v_mul_f32 v52, v52, v244\n"
    "v_mul_f32 v53, v53, v245\n"
    "v_mul_f32 v54, v54, v246\n"
    "v_mul_f32 v55, v55, v247\n"
    "v_mul_f32 v56, v56, v244\n"
    "v_mul_f32 v57, v57, v245\n"
    "v_mul_f32 v58, v58, v246\n"
    "v_mul_f32 v59, v59, v247\n"
    "v_mul_f32 v60, v60, v244\n"
    "v_mul_f32 v61, v61, v245\n"
    "v_mul_f32 v62, v62, v246\n"
    "v_mul_f32 v63, v63, v247\n"
    "s_nop 1\n"
    "v_mfma_f32_16x16x32_bf16 v[32:35], v[180:183], a[128:131], v[32:35]\n"
    "v_mfma_f32_16x16x32_bf16 v[36:39], v[180:183], a[132:135], v[36:39]\n"
    "v_mfma_f32_16x16x32_bf16 v[40:43], v[180:183], a[136:139], v[40:43]\n"
    "v_mfma_f32_16x16x32_bf16 v[44:47], v[180:183], a[140:143], v[44:47]\n"
    "v_mfma_f32_16x16x32_bf16 v[48:51], v[180:183], a[144:147], v[48:51]\n"
    "v_mfma_f32_16x16x32_bf16 v[52:55], v[180:183], a[148:151], v[52:55]\n"
    "v_mfma_f32_16x16x32_bf16 v[56:59], v[180:183], a[152:155], v[56:59]\n"
    "v_mfma_f32_16x16x32_bf16 v[60:63], v[180:183], a[156:159], v[60:63]\n"
    "v_mfma_f32_16x16x32_bf16 v[144:147], a[64:67], a[32:35], 0\n"
    "v_mfma_f32_16x16x32_bf16 v[144:147], a[68:71], a[36:39], v[144:147]\n"
    "v_mfma_f32_16x16x32_bf16 v[144:147], a[72:75], a[40:43], v[144:147]\n"
    "v_mfma_f32_16x16x32_bf16 v[144:147], a[76:79], a[44:47], v[144:147]\n"
    "v_mfma_f32_16x16x32_bf16 v[148:151], a[80:83], a[32:35], 0\n"
    "v_mfma_f32_16x16x32_bf16 v[148:151], a[84:87], a[36:39], v[148:151]\n"
    "v_mfma_f32_16x16x32_bf16 v[148:151], a[88:91], a[40:43], v[148:151]\n"
    "v_mfma_f32_16x16x32_bf16 v[148:151], a[92:95], a[44:47], v[148:151]\n"
    "s_nop 7\n"
    "v_max3_f32 v204, v144, v145, v146\n"
    "v_max3_f32 v204, v204, v147, v148\n"
    "v_max3_f32 v204, v204, v149, v150\n"
    "v_max_f32 v204, v204, v151\n"
    "v_mov_b32 v205, v204\n"
    "s_nop 1\n"
    "v_permlane16_swap_b32 v204, v205\n"
    "v_max_f32 v204, v204, v205\n"
    "v_mov_b32 v205, v204\n"
    "s_nop 1\n"
    "v_permlane32_swap_b32 v204, v205\n"
    "v_max_f32 v204, v204, v205\n"
    "v_max_f32 v204, v204, v250\n"
    "v_sub_f32 v206, v250, v204\n"
    "v_exp_f32 v206, v206\n"
    "v_mov_b32 v250, v204\n"
    "v_mul_f32 v194, v194, v206\n"
    "ds_write_b32 v224, v206 offset:0\n"
    "v_sub_f32 v144, v144, v204\n"
    "v_sub_f32 v145, v145, v204\n"
    "v_sub_f32 v146, v146, v204\n"
    "v_sub_f32 v147, v147, v204\n"
    "v_sub_f32 v148, v148, v204\n"
    "v_sub_f32 v149, v149, v204\n"
    "v_sub_f32 v150, v150, v204\n"
    "v_sub_f32 v151, v151, v204\n"
    "v_exp_f32 v144, v144\n"
    "v_exp_f32 v145, v145\n"
    "v_exp_f32 v146, v146\n"
    "v_exp_f32 v147, v147\n"
    "v_exp_f32 v148, v148\n"
    "v_exp_f32 v149, v149\n"
    "v_exp_f32 v150, v150\n"
    "v_exp_f32 v151, v151\n"
    "v_add_f32 v194, v194, v144\n"
    "v_add_f32 v194, v194, v145\n"
    "v_add_f32 v194, v194, v146\n"
    "v_add_f32 v194, v194, v147\n"
    "v_add_f32 v194, v194, v148\n"
    "v_add_f32 v194, v194, v149\n"
    "v_add_f32 v194, v194, v150\n"
    "v_add_f32 v194, v194, v151\n"
    "v_cvt_pk_bf16_f32 v184, v144, v145\n"
    "v_cvt_pk_bf16_f32 v185, v146, v147\n"
    "v_cvt_pk_bf16_f32 v186, v148, v149\n"
    "v_cvt_pk_bf16_f32 v187, v150, v151\n"
    "s_waitcnt lgkmcnt(0)\n"
    "ds_read_b128 v[244:247], v225 offset:0\n"
    "s_waitcnt lgkmcnt(0)\n"
    "v_mul_f32 v64, v64, v244\n"
    "v_mul_f32 v65, v65, v245\n"
    "v_mul_f32 v66, v66, v246\n"
    "v_mul_f32 v67, v67, v247\n"
    "v_mul_f32 v68, v68, v244\n"
    "v_mul_f32 v69, v69, v245\n"
    "v_mul_f32 v70, v70, v246\n"
    "v_mul_f32 v71, v71, v247\n"
    "v_mul_f32 v72, v72, v244\n"
    "v_mul_f32 v73, v73, v245\n"
    "v_mul_f32 v74, v74, v246\n"
    "v_mul_f32 v75, v75, v247\n"
    "v_mul_f32 v76, v76, v244\n"
    "v_mul_f32 v77, v77, v245\n"
    "v_mul_f32 v78, v78, v246\n"
    "v_mul_f32 v79, v79, v247\n"
    "v_mul_f32 v80, v80, v244\n"
    "v_mul_f32 v81, v81, v245\n"
    "v_mul_f32 v82, v82, v246\n"
    "v_mul_f32 v83, v83, v247\n"
    "v_mul_f32 v84, v84, v244\n"
    "v_mul_f32 v85, v85, v245\n"
    "v_mul_f32 v86, v86, v246\n"
    "v_mul_f32 v87, v87, v247\n"
    "v_mul_f32 v88, v88, v244\n"
    "v_mul_f32 v89, v89, v245\n"
    "v_mul_f32 v90, v90, v246\n"
    "v_mul_f32 v91, v91, v247\n"
    "v_mul_f32 v92, v92, v244\n"
    "v_mul_f32 v93, v93, v245\n"
    "v_mul_f32 v94, v94, v246\n"
    "v_mul_f32 v95, v95, v247\n"
    "s_nop 1\n"
    "v_mfma_f32_16x16x32_bf16 v[64:67], v[184:187], a[128:131], v[64:67]\n"
    "v_mfma_f32_16x16x32_bf16 v[68:71], v[184:187], a[132:135], v[68:71]\n"
    "v_mfma_f32_16x16x32_bf16 v[72:75], v[184:187], a[136:139], v[72:75]\n"
    "v_mfma_f32_16x16x32_bf16 v[76:79], v[184:187], a[140:143], v[76:79]\n"
    "v_mfma_f32_16x16x32_bf16 v[80:83], v[184:187], a[144:147], v[80:83]\n"
    "v_mfma_f32_16x16x32_bf16 v[84:87], v[184:187], a[148:151], v[84:87]\n"
    "v_mfma_f32_16x16x32_bf16 v[88:91], v[184:187], a[152:155], v[88:91]\n"
    "v_mfma_f32_16x16x32_bf16 v[92:95], v[184:187], a[156:159], v[92:95]\n"
    "v_mfma_f32_16x16x32_bf16 v[152:155], a[64:67], a[48:51], 0\n"
    "v_mfma_f32_16x16x32_bf16 v[152:155], a[68:71], a[52:55], v[152:155]\n"
    "v_mfma_f32_16x16x32_bf16 v[152:155], a[72:75], a[56:59], v[152:155]\n"
    "v_mfma_f32_16x16x32_bf16 v[152:155], a[76:79], a[60:63], v[152:155]\n"
    "v_mfma_f32_16x16x32_bf16 v[156:159], a[80:83], a[48:51], 0\n"
    "v_mfma_f32_16x16x32_bf16 v[156:159], a[84:87], a[52:55], v[156:159]\n"
    "v_mfma_f32_16x16x32_bf16 v[156:159], a[88:91], a[56:59], v[156:159]\n"
    "v_mfma_f32_16x16x32_bf16 v[156:159], a[92:95], a[60:63], v[156:159]\n"
    "s_nop 7\n"
    "v_max3_f32 v204, v152, v153, v154\n"
    "v_max3_f32 v204, v204, v155, v156\n"
    "v_max3_f32 v204, v204, v157, v158\n"
    "v_max_f32 v204, v204, v159\n"
    "v_mov_b32 v205, v204\n"
    "s_nop 1\n"
    "v_permlane16_swap_b32 v204, v205\n"
    "v_max_f32 v204, v204, v205\n"
    "v_mov_b32 v205, v204\n"
    "s_nop 1\n"
    "v_permlane32_swap_b32 v204, v205\n"
    "v_max_f32 v204, v204, v205\n"
    "v_max_f32 v204, v204, v251\n"
    "v_sub_f32 v206, v251, v204\n"
    "v_exp_f32 v206, v206\n"
    "v_mov_b32 v251, v204\n"
    "v_mul_f32 v195, v195, v206\n"
    "ds_write_b32 v224, v206 offset:0\n"
    "v_sub_f32 v152, v152, v204\n"
    "v_sub_f32 v153, v153, v204\n"
    "v_sub_f32 v154, v154, v204\n"
    "v_sub_f32 v155, v155, v204\n"
    "v_sub_f32 v156, v156, v204\n"
    "v_sub_f32 v157, v157, v204\n"
    "v_sub_f32 v158, v158, v204\n"
    "v_sub_f32 v159, v159, v204\n"
    "v_exp_f32 v152, v152\n"
    "v_exp_f32 v153, v153\n"
    "v_exp_f32 v154, v154\n"
    "v_exp_f32 v155, v155\n"
    "v_exp_f32 v156, v156\n"
    "v_exp_f32 v157, v157\n"
    "v_exp_f32 v158, v158\n"
    "v_exp_f32 v159, v159\n"
    "v_add_f32 v195, v195, v152\n"
    "v_add_f32 v195, v195, v153\n"
    "v_add_f32 v195, v195, v154\n"
    "v_add_f32 v195, v195, v155\n"
    "v_add_f32 v195, v195, v156\n"
    "v_add_f32 v195, v195, v157\n"
    "v_add_f32 v195, v195, v158\n"
    "v_add_f32 v195, v195, v159\n"
    "v_cvt_pk_bf16_f32 v188, v152, v153\n"
    "v_cvt_pk_bf16_f32 v189, v154, v155\n"
    "v_cvt_pk_bf16_f32 v190, v156, v157\n"
    "v_cvt_pk_bf16_f32 v191, v158, v159\n"
    "s_waitcnt lgkmcnt(0)\n"
    "ds_read_b128 v[244:247], v225 offset:0\n"
    "s_waitcnt lgkmcnt(0)\n"
    "v_mul_f32 v96, v96, v244\n"
    "v_mul_f32 v97, v97, v245\n"
    "v_mul_f32 v98, v98, v246\n"
    "v_mul_f32 v99, v99, v247\n"
    "v_mul_f32 v100, v100, v244\n"
    "v_mul_f32 v101, v101, v245\n"
    "v_mul_f32 v102, v102, v246\n"
    "v_mul_f32 v103, v103, v247\n"
    "v_mul_f32 v104, v104, v244\n"
    "v_mul_f32 v105, v105, v245\n"
    "v_mul_f32 v106, v106, v246\n"
    "v_mul_f32 v107, v107, v247\n"
    "v_mul_f32 v108, v108, v244\n"
    "v_mul_f32 v109, v109, v245\n"
    "v_mul_f32 v110, v110, v246\n"
    "v_mul_f32 v111, v111, v247\n"
    "v_mul_f32 v112, v112, v244\n"
    "v_mul_f32 v113, v113, v245\n"
    "v_mul_f32 v114, v114, v246\n"
    "v_mul_f32 v115, v115, v247\n"
    "v_mul_f32 v116, v116, v244\n"
    "v_mul_f32 v117, v117, v245\n"
    "v_mul_f32 v118, v118, v246\n"
    "v_mul_f32 v119, v119, v247\n"
    "v_mul_f32 v120, v120, v244\n"
    "v_mul_f32 v121, v121, v245\n"
    "v_mul_f32 v122, v122, v246\n"
    "v_mul_f32 v123, v123, v247\n"
    "v_mul_f32 v124, v124, v244\n"
    "v_mul_f32 v125, v125, v245\n"
    "v_mul_f32 v126, v126, v246\n"
    "v_mul_f32 v127, v127, v247\n"
    "s_nop 1\n"
    "v_mfma_f32_16x16x32_bf16 v[96:99], v[188:191], a[128:131], v[96:99]\n"
    "v_mfma_f32_16x16x32_bf16 v[100:103], v[188:191], a[132:135], v[100:103]\n"
    "v_mfma_f32_16x16x32_bf16 v[104:107], v[188:191], a[136:139], v[104:107]\n"
    "v_mfma_f32_16x16x32_bf16 v[108:111], v[188:191], a[140:143], v[108:111]\n"
    "v_mfma_f32_16x16x32_bf16 v[112:115], v[188:191], a[144:147], v[112:115]\n"
    "v_mfma_f32_16x16x32_bf16 v[116:119], v[188:191], a[148:151], v[116:119]\n"
    "v_mfma_f32_16x16x32_bf16 v[120:123], v[188:191], a[152:155], v[120:123]\n"
    "v_mfma_f32_16x16x32_bf16 v[124:127], v[188:191], a[156:159], v[124:127]\n"
    "s_nop 7\n"
    "s_add_u32 s57, s57, 0x4000\n"
    "s_cmp_lt_u32 s57, 1048576\n"
    "s_cbranch_scc1 .Lfb_loop\n"
    "s_nop 7\n"
    "v_mov_b32 v204, v192\n"
    "v_mov_b32 v205, v204\n"
    "s_nop 1\n"
    "v_permlane16_swap_b32 v204, v205\n"
    "v_add_f32 v204, v204, v205\n"
    "v_mov_b32 v205, v204\n"
    "s_nop 1\n"
    "v_permlane32_swap_b32 v204, v205\n"
    "v_add_f32 v204, v204, v205\n"
    "v_rcp_f32 v204, v204\n"
    "s_nop 0\n"
    "ds_write_b32 v224, v204 offset:0\n"
    "v_mov_b32 v206, v193\n"
    "v_mov_b32 v207, v206\n"
    "s_nop 1\n"
    "v_permlane16_swap_b32 v206, v207\n"
    "v_add_f32 v206, v206, v207\n"
    "v_mov_b32 v207, v206\n"
    "s_nop 1\n"
    "v_permlane32_swap_b32 v206, v207\n"
    "v_add_f32 v206, v206, v207\n"
    "v_rcp_f32 v206, v206\n"
    "s_nop 0\n"
    "ds_write_b32 v224, v206 offset:64\n"
    "v_mov_b32 v208, v194\n"
    "v_mov_b32 v209, v208\n"
    "s_nop 1\n"
    "v_permlane16_swap_b32 v208, v209\n"
    "v_add_f32 v208, v208, v209\n"
    "v_mov_b32 v209, v208\n"
    "s_nop 1\n"
    "v_permlane32_swap_b32 v208, v209\n"
    "v_add_f32 v208, v208, v209\n"
    "v_rcp_f32 v208, v208\n"
    "s_nop 0\n"
    "ds_write_b32 v224, v208 offset:128\n"
    "v_mov_b32 v210, v195\n"
    "v_mov_b32 v211, v210\n"
    "s_nop 1\n"
    "v_permlane16_swap_b32 v210, v211\n"
    "v_add_f32 v210, v210, v211\n"
    "v_mov_b32 v211, v210\n"
    "s_nop 1\n"
    "v_permlane32_swap_b32 v210, v211\n"
    "v_add_f32 v210, v210, v211\n"
    "v_rcp_f32 v210, v210\n"
    "s_nop 0\n"
    "ds_write_b32 v224, v210 offset:192\n"
    "s_waitcnt lgkmcnt(0)\n"
    "ds_read_b128 v[160:163], v225 offset:0\n"
    "ds_read_b128 v[164:167], v225 offset:64\n"
    "ds_read_b128 v[168:171], v225 offset:128\n"
    "ds_read_b128 v[172:175], v225 offset:192\n"
    "s_waitcnt lgkmcnt(0)\n"
    "v_pk_mul_f32 v[0:1], v[0:1], v[160:161]\n"
    "v_pk_mul_f32 v[4:5], v[4:5], v[160:161]\n"
    "v_pk_mul_f32 v[8:9], v[8:9], v[160:161]\n"
    "v_pk_mul_f32 v[12:13], v[12:13], v[160:161]\n"
    "v_pk_mul_f32 v[16:17], v[16:17], v[160:161]\n"
    "v_pk_mul_f32 v[20:21], v[20:21], v[160:161]\n"
    "v_pk_mul_f32 v[24:25], v[24:25], v[160:161]\n"
    "v_pk_mul_f32 v[28:29], v[28:29], v[160:161]\n"
    "v_cvt_pk_bf16_f32 v228, v0, v4\n"
    "v_cvt_pk_bf16_f32 v229, v8, v12\n"
    "v_cvt_pk_bf16_f32 v230, v16, v20\n"
    "v_cvt_pk_bf16_f32 v231, v24, v28\n"
    "buffer_store_dwordx4 v[228:231], v226, s[44:47], s80 offen offset:0 sc1\n"
    "v_cvt_pk_bf16_f32 v232, v1, v5\n"
    "v_cvt_pk_bf16_f32 v233, v9, v13\n"
    "v_cvt_pk_bf16_f32 v234, v17, v21\n"
    "v_cvt_pk_bf16_f32 v235, v25, v29\n"
    "buffer_store_dwordx4 v[232:235], v226, s[44:47], s80 offen offset:256 sc1\n"
    "v_pk_mul_f32 v[2:3], v[2:3], v[162:163]\n"
    "v_pk_mul_f32 v[6:7], v[6:7], v[162:163]\n"
    "v_pk_mul_f32 v[10:11], v[10:11], v[162:163]\n"
    "v_pk_mul_f32 v[14:15], v[14:15], v[162:163]\n"
    "v_pk_mul_f32 v[18:19], v[18:19], v[162:163]\n"
    "v_pk_mul_f32 v[22:23], v[22:23], v[162:163]\n"
    "v_pk_mul_f32 v[26:27], v[26:27], v[162:163]\n"
    "v_pk_mul_f32 v[30:31], v[30:31], v[162:163]\n"
    "v_cvt_pk_bf16_f32 v236, v2, v6\n"
    "v_cvt_pk_bf16_f32 v237, v10, v14\n"
    "v_cvt_pk_bf16_f32 v238, v18, v22\n"
    "v_cvt_pk_bf16_f32 v239, v26, v30\n"
    "buffer_store_dwordx4 v[236:239], v226, s[44:47], s80 offen offset:512 sc1\n"
    "v_cvt_pk_bf16_f32 v240, v3, v7\n"
    "v_cvt_pk_bf16_f32 v241, v11, v15\n"
    "v_cvt_pk_bf16_f32 v242, v19, v23\n"
    "v_cvt_pk_bf16_f32 v243, v27, v31\n"
    "buffer_store_dwordx4 v[240:243], v226, s[44:47], s80 offen offset:768 sc1\n"
    "v_pk_mul_f32 v[32:33], v[32:33], v[164:165]\n"
    "v_pk_mul_f32 v[36:37], v[36:37], v[164:165]\n"
    "v_pk_mul_f32 v[40:41], v[40:41], v[164:165]\n"
    "v_pk_mul_f32 v[44:45], v[44:45], v[164:165]\n"
    "v_pk_mul_f32 v[48:49], v[48:49], v[164:165]\n"
    "v_pk_mul_f32 v[52:53], v[52:53], v[164:165]\n"
    "v_pk_mul_f32 v[56:57], v[56:57], v[164:165]\n"
    "v_pk_mul_f32 v[60:61], v[60:61], v[164:165]\n"
    "v_cvt_pk_bf16_f32 v228, v32, v36\n"
    "v_cvt_pk_bf16_f32 v229, v40, v44\n"
    "v_cvt_pk_bf16_f32 v230, v48, v52\n"
    "v_cvt_pk_bf16_f32 v231, v56, v60\n"
    "buffer_store_dwordx4 v[228:231], v226, s[44:47], s81 offen offset:0 sc1\n"
    "v_cvt_pk_bf16_f32 v232, v33, v37\n"
    "v_cvt_pk_bf16_f32 v233, v41, v45\n"
    "v_cvt_pk_bf16_f32 v234, v49, v53\n"
    "v_cvt_pk_bf16_f32 v235, v57, v61\n"
    "buffer_store_dwordx4 v[232:235], v226, s[44:47], s81 offen offset:256 sc1\n"
    "v_pk_mul_f32 v[34:35], v[34:35], v[166:167]\n"
    "v_pk_mul_f32 v[38:39], v[38:39], v[166:167]\n"
    "v_pk_mul_f32 v[42:43], v[42:43], v[166:167]\n"
    "v_pk_mul_f32 v[46:47], v[46:47], v[166:167]\n"
    "v_pk_mul_f32 v[50:51], v[50:51], v[166:167]\n"
    "v_pk_mul_f32 v[54:55], v[54:55], v[166:167]\n"
    "v_pk_mul_f32 v[58:59], v[58:59], v[166:167]\n"
    "v_pk_mul_f32 v[62:63], v[62:63], v[166:167]\n"
    "v_cvt_pk_bf16_f32 v236, v34, v38\n"
    "v_cvt_pk_bf16_f32 v237, v42, v46\n"
    "v_cvt_pk_bf16_f32 v238, v50, v54\n"
    "v_cvt_pk_bf16_f32 v239, v58, v62\n"
    "buffer_store_dwordx4 v[236:239], v226, s[44:47], s81 offen offset:512 sc1\n"
    "v_cvt_pk_bf16_f32 v240, v35, v39\n"
    "v_cvt_pk_bf16_f32 v241, v43, v47\n"
    "v_cvt_pk_bf16_f32 v242, v51, v55\n"
    "v_cvt_pk_bf16_f32 v243, v59, v63\n"
    "buffer_store_dwordx4 v[240:243], v226, s[44:47], s81 offen offset:768 sc1\n"
    "v_pk_mul_f32 v[64:65], v[64:65], v[168:169]\n"
    "v_pk_mul_f32 v[68:69], v[68:69], v[168:169]\n"
    "v_pk_mul_f32 v[72:73], v[72:73], v[168:169]\n"
    "v_pk_mul_f32 v[76:77], v[76:77], v[168:169]\n"
    "v_pk_mul_f32 v[80:81], v[80:81], v[168:169]\n"
    "v_pk_mul_f32 v[84:85], v[84:85], v[168:169]\n"
    "v_pk_mul_f32 v[88:89], v[88:89], v[168:169]\n"
    "v_pk_mul_f32 v[92:93], v[92:93], v[168:169]\n"
    "v_cvt_pk_bf16_f32 v228, v64, v68\n"
    "v_cvt_pk_bf16_f32 v229, v72, v76\n"
    "v_cvt_pk_bf16_f32 v230, v80, v84\n"
    "v_cvt_pk_bf16_f32 v231, v88, v92\n"
    "buffer_store_dwordx4 v[228:231], v226, s[44:47], s82 offen offset:0 sc1\n"
    "v_cvt_pk_bf16_f32 v232, v65, v69\n"
    "v_cvt_pk_bf16_f32 v233, v73, v77\n"
    "v_cvt_pk_bf16_f32 v234, v81, v85\n"
    "v_cvt_pk_bf16_f32 v235, v89, v93\n"
    "buffer_store_dwordx4 v[232:235], v226, s[44:47], s82 offen offset:256 sc1\n"
    "v_pk_mul_f32 v[66:67], v[66:67], v[170:171]\n"
    "v_pk_mul_f32 v[70:71], v[70:71], v[170:171]\n"
    "v_pk_mul_f32 v[74:75], v[74:75], v[170:171]\n"
    "v_pk_mul_f32 v[78:79], v[78:79], v[170:171]\n"
    "v_pk_mul_f32 v[82:83], v[82:83], v[170:171]\n"
    "v_pk_mul_f32 v[86:87], v[86:87], v[170:171]\n"
    "v_pk_mul_f32 v[90:91], v[90:91], v[170:171]\n"
    "v_pk_mul_f32 v[94:95], v[94:95], v[170:171]\n"
    "v_cvt_pk_bf16_f32 v236, v66, v70\n"
    "v_cvt_pk_bf16_f32 v237, v74, v78\n"
    "v_cvt_pk_bf16_f32 v238, v82, v86\n"
    "v_cvt_pk_bf16_f32 v239, v90, v94\n"
    "buffer_store_dwordx4 v[236:239], v226, s[44:47], s82 offen offset:512 sc1\n"
    "v_cvt_pk_bf16_f32 v240, v67, v71\n"
    "v_cvt_pk_bf16_f32 v241, v75, v79\n"
    "v_cvt_pk_bf16_f32 v242, v83, v87\n"
    "v_cvt_pk_bf16_f32 v243, v91, v95\n"
    "buffer_store_dwordx4 v[240:243], v226, s[44:47], s82 offen offset:768 sc1\n"
    "v_pk_mul_f32 v[96:97], v[96:97], v[172:173]\n"
    "v_pk_mul_f32 v[100:101], v[100:101], v[172:173]\n"
    "v_pk_mul_f32 v[104:105], v[104:105], v[172:173]\n"
    "v_pk_mul_f32 v[108:109], v[108:109], v[172:173]\n"
    "v_pk_mul_f32 v[112:113], v[112:113], v[172:173]\n"
    "v_pk_mul_f32 v[116:117], v[116:117], v[172:173]\n"
    "v_pk_mul_f32 v[120:121], v[120:121], v[172:173]\n"
    "v_pk_mul_f32 v[124:125], v[124:125], v[172:173]\n"
    "v_cvt_pk_bf16_f32 v228, v96, v100\n"
    "v_cvt_pk_bf16_f32 v229, v104, v108\n"
    "v_cvt_pk_bf16_f32 v230, v112, v116\n"
    "v_cvt_pk_bf16_f32 v231, v120, v124\n"
    "buffer_store_dwordx4 v[228:231], v226, s[44:47], s83 offen offset:0 sc1\n"
    "v_cvt_pk_bf16_f32 v232, v97, v101\n"
    "v_cvt_pk_bf16_f32 v233, v105, v109\n"
    "v_cvt_pk_bf16_f32 v234, v113, v117\n"
    "v_cvt_pk_bf16_f32 v235, v121, v125\n"
    "buffer_store_dwordx4 v[232:235], v226, s[44:47], s83 offen offset:256 sc1\n"
    "v_pk_mul_f32 v[98:99], v[98:99], v[174:175]\n"
    "v_pk_mul_f32 v[102:103], v[102:103], v[174:175]\n"
    "v_pk_mul_f32 v[106:107], v[106:107], v[174:175]\n"
    "v_pk_mul_f32 v[110:111], v[110:111], v[174:175]\n"
    "v_pk_mul_f32 v[114:115], v[114:115], v[174:175]\n"
    "v_pk_mul_f32 v[118:119], v[118:119], v[174:175]\n"
    "v_pk_mul_f32 v[122:123], v[122:123], v[174:175]\n"
    "v_pk_mul_f32 v[126:127], v[126:127], v[174:175]\n"
    "v_cvt_pk_bf16_f32 v236, v98, v102\n"
    "v_cvt_pk_bf16_f32 v237, v106, v110\n"
    "v_cvt_pk_bf16_f32 v238, v114, v118\n"
    "v_cvt_pk_bf16_f32 v239, v122, v126\n"
    "buffer_store_dwordx4 v[236:239], v226, s[44:47], s83 offen offset:512 sc1\n"
    "v_cvt_pk_bf16_f32 v240, v99, v103\n"
    "v_cvt_pk_bf16_f32 v241, v107, v111\n"
    "v_cvt_pk_bf16_f32 v242, v115, v119\n"
    "v_cvt_pk_bf16_f32 v243, v123, v127\n"
    "buffer_store_dwordx4 v[240:243], v226, s[44:47], s83 offen offset:768 sc1\n"
    "s_branch .Lfin\n"
    ".Lend:\n"
  : : [q] "s"(q), [kv] "s"(kv), [o] "s"(o), [wave] "s"(wave), [lds] "s"(ldsb), [j] "s"(j),
      [srck] "s"(qk + so), [sck] "s"(ks + ko), [zrk] "s"(kz + ko), [srcv] "s"(qv + so), [scv] "s"(vs + vo), [zrv] "s"(vz + vo),
      [ctl] "s"(ctl)
  : "memory", "vcc", "scc", "v0", "v1", "v2", "v3", "v4", "v5", "v6", "v7", "v8", "v9", "v10", "v11", "v12", "v13", "v14", "v15", "v16", "v17", "v18", "v19", "v20", "v21", "v22", "v23", "v24", "v25", "v26", "v27", "v28", "v29", "v30", "v31", "v32", "v33", "v34", "v35", "v36", "v37", "v38", "v39", "v40", "v41", "v42", "v43", "v44", "v45", "v46", "v47", "v48", "v49", "v50", "v51", "v52", "v53", "v54", "v55", "v56", "v57", "v58", "v59", "v60", "v61", "v62", "v63", "v64", "v65", "v66", "v67", "v68", "v69", "v70", "v71", "v72", "v73", "v74", "v75", "v76", "v77", "v78", "v79", "v80", "v81", "v82", "v83", "v84", "v85", "v86", "v87", "v88", "v89", "v90", "v91", "v92", "v93", "v94", "v95", "v96", "v97", "v98", "v99", "v100", "v101", "v102", "v103", "v104", "v105", "v106", "v107", "v108", "v109", "v110", "v111", "v112", "v113", "v114", "v115", "v116", "v117", "v118", "v119", "v120", "v121", "v122", "v123", "v124", "v125", "v126", "v127", "v128", "v129", "v130", "v131", "v132", "v133", "v134", "v135", "v136", "v137", "v138", "v139", "v140", "v141", "v142", "v143", "v144", "v145", "v146", "v147", "v148", "v149", "v150", "v151", "v152", "v153", "v154", "v155", "v156", "v157", "v158", "v159", "v160", "v161", "v162", "v163", "v164", "v165", "v166", "v167", "v168", "v169", "v170", "v171", "v172", "v173", "v174", "v175", "v176", "v177", "v178", "v179", "v180", "v181", "v182", "v183", "v184", "v185", "v186", "v187", "v188", "v189", "v190", "v191", "v192", "v193", "v194", "v195", "v196", "v197", "v198", "v199", "v200", "v201", "v202", "v203", "v204", "v205", "v206", "v207", "v208", "v209", "v210", "v211", "v212", "v213", "v214", "v215", "v216", "v217", "v218", "v219", "v220", "v221", "v222", "v223", "v224", "v225", "v226", "v227", "v228", "v229", "v230", "v231", "v232", "v233", "v234", "v235", "v236", "v237", "v238", "v239", "v240", "v241", "v242", "v243", "v244", "v245", "v246", "v247", "v248", "v249", "v250", "v251", "v252", "v253", "v254", "v255", "a0", "a1", "a2", "a3", "a4", "a5", "a6", "a7", "a8", "a9", "a10", "a11", "a12", "a13", "a14", "a15", "a16", "a17", "a18", "a19", "a20", "a21", "a22", "a23", "a24", "a25", "a26", "a27", "a28", "a29", "a30", "a31", "a32", "a33", "a34", "a35", "a36", "a37", "a38", "a39", "a40", "a41", "a42", "a43", "a44", "a45", "a46", "a47", "a48", "a49", "a50", "a51", "a52", "a53", "a54", "a55", "a56", "a57", "a58", "a59", "a60", "a61", "a62", "a63", "a64", "a65", "a66", "a67", "a68", "a69", "a70", "a71", "a72", "a73", "a74", "a75", "a76", "a77", "a78", "a79", "a80", "a81", "a82", "a83", "a84", "a85", "a86", "a87", "a88", "a89", "a90", "a91", "a92", "a93", "a94", "a95", "a96", "a97", "a98", "a99", "a100", "a101", "a102", "a103", "a104", "a105", "a106", "a107", "a108", "a109", "a110", "a111", "a112", "a113", "a114", "a115", "a116", "a117", "a118", "a119", "a120", "a121", "a122", "a123", "a124", "a125", "a126", "a127", "a128", "a129", "a130", "a131", "a132", "a133", "a134", "a135", "a136", "a137", "a138", "a139", "a140", "a141", "a142", "a143", "a144", "a145", "a146", "a147", "a148", "a149", "a150", "a151", "a152", "a153", "a154", "a155", "a156", "a157", "a158", "a159", "a160", "a161", "a162", "a163", "a164", "a165", "a166", "a167", "a168", "a169", "a170", "a171", "a172", "a173", "a174", "a175", "a176", "a177", "a178", "a179", "a180", "a181", "a182", "a183", "a184", "a185", "a186", "a187", "a188", "a189", "a190", "a191", "a192", "a193", "a194", "a195", "a196", "a197", "a198", "a199", "a200", "a201", "a202", "a203", "a204", "a205", "a206", "a207", "a208", "a209", "a210", "a211", "a212", "a213", "a214", "a215", "a216", "a217", "a218", "a219", "a220", "a221", "a222", "a223", "s38", "s39", "s40", "s41", "s42", "s43", "s44", "s45", "s46", "s47", "s48", "s49", "s50", "s51", "s52", "s53", "s54", "s55", "s56", "s57", "s58", "s59", "s60", "s61", "s62", "s63", "s64", "s65", "s66", "s67", "s68", "s69", "s70", "s71", "s72", "s73", "s74", "s75", "s76", "s77", "s78", "s79", "s80", "s81", "s82", "s83", "s84", "s85", "s86", "s87", "s88", "s89", "s90", "s91", "s92", "s93", "s94", "s95");
}

extern "C" void kernel_launch(void* const* d_in, const int* in_sizes, int n_in, void* d_out, int out_size,
                              void* d_ws, size_t ws_size, hipStream_t stream) {
  hipLaunchKernelGGL(attn_fwd, dim3(256), dim3(256), 0, stream, (const char*)d_in[0], (const char*)d_in[1], (const char*)d_in[2],
                     (const char*)d_in[3], (const char*)d_in[4], (const char*)d_in[5], (const char*)d_in[6], (char*)d_ws, (char*)d_out);
}
```
